```python
import torch

def _softmax(x, axis=-1):
    return torch.softmax(x, dim=axis)
def _gelu(x):
    return torch.nn.functional.gelu(x, approximate="tanh")
def _silu(x):
    return torch.nn.functional.silu(x)
def _sigmoid(x):
    return torch.sigmoid(x)
def _layernorm(x, axis=-1, eps=1e-5):
    m = x.mean(dim=axis, keepdim=True)
    v = x.var(dim=axis, keepdim=True, unbiased=False)
    return (x - m) / torch.sqrt(v + eps)
def _scatter_add(tgt, idx, src, axis=0):
    return tgt.clone().index_add_(axis, idx, src)
def _segment_sum(src, seg, n):
    out = torch.zeros((n,) + src.shape[1:], dtype=src.dtype)
    return out.index_add_(0, seg, src)
def _pad(x, pw):
    flat = tuple(v for pair in reversed(pw) for v in pair)
    return torch.nn.functional.pad(x, flat)

kernel_name = 'concat_heads__ssm_conv_s4__mha_block__prompt'

def setup_inputs():

    _w = (lambda a: (lambda t: t.to(torch.bfloat16) if t.is_floating_point() else t)(torch.as_tensor(a)))
    def _A_setup():
        B, L, D, N = 1, 2048, 512, 128
        A_re = -(lambda _x: torch.exp(torch.as_tensor(_x)))((lambda *s: torch.randn(*s, dtype=torch.bfloat16) * 6.4)(D, N))
        A_im = torch.pi * (lambda *s: torch.randn(*s, dtype=torch.bfloat16) * 0.1)(D, N)
        B_re = (lambda *s: torch.randn(*s, dtype=torch.bfloat16) * 0.2)(D, N)
        B_im = (lambda *s: torch.randn(*s, dtype=torch.bfloat16) * 0.2)(D, N)
        C_re = (lambda *s: torch.randn(*s, dtype=torch.bfloat16) * 0.2)(D, N)
        C_im = (lambda *s: torch.randn(*s, dtype=torch.bfloat16) * 0.2)(D, N)
        log_dt = (lambda *s: torch.randn(*s, dtype=torch.bfloat16) * 0.1)(D) * 0.5 - 4.0
        Dskip = (lambda *s: torch.randn(*s, dtype=torch.bfloat16) * 0.1)(D)
        return {
            "u": _w((lambda *s: torch.randn(*s, dtype=torch.bfloat16) * 0.05)(B, L, D)),
            "A_re": _w(A_re),
            "A_im": _w(A_im),
            "B_re": _w(B_re),
            "B_im": _w(B_im),
            "C_re": _w(C_re),
            "C_im": _w(C_im),
            "log_dt": _w(log_dt),
            "Dskip": _w(Dskip),
        }
    _A_in = _A_setup()

    def _B_setup():
        B, S, D, H = 1, 2048, 512, 32
        return {
            "x": _w((lambda *s: torch.randn(*s, dtype=torch.bfloat16) * 0.2)(B, S, D)),
            "wqkv": _w((lambda *s: torch.randn(*s, dtype=torch.bfloat16) * 0.2)(D, 3 * D)),
            "wo": _w((lambda *s: torch.randn(*s, dtype=torch.bfloat16) * 0.05)(D, D)),
        }
    _B_in = _B_setup()

    return {
        "A_u": _A_in["u"],
        "A_A_re": _A_in["A_re"],
        "A_A_im": _A_in["A_im"],
        "A_B_re": _A_in["B_re"],
        "A_B_im": _A_in["B_im"],
        "A_C_re": _A_in["C_re"],
        "A_C_im": _A_in["C_im"],
        "A_log_dt": _A_in["log_dt"],
        "A_Dskip": _A_in["Dskip"],
        "B_x": _B_in["x"],
        "B_wqkv": _B_in["wqkv"],
        "B_wo": _B_in["wo"],
    }

def reference(A_u, A_A_re, A_A_im, A_B_re, A_B_im, A_C_re, A_C_im, A_log_dt, A_Dskip, B_x, B_wqkv, B_wo):
    def _A_ref(u, A_re, A_im, B_re, B_im, C_re, C_im, log_dt, Dskip):
        Bsz, L, D = u.shape
        dt = torch.exp(log_dt)[:, None]
        A = A_re + 1j * A_im
        Bc = B_re + 1j * B_im
        Cc = C_re + 1j * C_im
        logdA = dt * A
        dB = dt * Bc
        l = (lambda x, dt: x.to(dt))(torch.arange(L), A_re.dtype)
        P = torch.exp(logdA[:, :, None] * l[None, None, :])
        K = torch.einsum("dn,dnl,dn->dl", Cc, P, dB).real
        L2 = 2 * L
        Kp = _pad(K, ((0, 0), (0, L)))
        up = (lambda x, *d: x.permute(*d))(u, 0, 2, 1)
        up = _pad(up, ((0, 0), (0, 0), (0, L)))
        Kf = (lambda x, axes=(-2,-1): torch.fft.rfft2(x.float(), dim=axes))(Kp[:, None, :], axes=(-2, -1))
        Uf = (lambda x, axes=(-2,-1): torch.fft.rfft2(x.float(), dim=axes))(up[:, :, None, :], axes=(-2, -1))
        Yf = Uf * Kf[None, :, :, :]
        y = (lambda x, s, axes=(-2,-1): torch.fft.irfft2(x, s=s, dim=axes).to(torch.bfloat16))(Yf, (1, L2), axes=(-2, -1))[:, :, 0, :L]
        y = (lambda x, *d: x.permute(*d))(y, 0, 2, 1) + u * Dskip[None, None, :]
        return y

    def _B_ref(x, wqkv, wo):
        B, S, D = x.shape
        H = 32
        Dh = D // H
        qkv = (lambda x, *d: x.permute(*d))((x @ wqkv).reshape(B, S, 3, H, Dh), 2, 0, 3, 1, 4)
        q, k, v = qkv[0], qkv[1], qkv[2]
        scores = torch.einsum("bhqd,bhkd->bhqk", q, k) * (Dh ** -0.5)
        p = _softmax(scores, axis=-1)
        o = torch.einsum("bhqk,bhkd->bhqd", p, v)
        return (lambda x, *d: x.permute(*d))(o, 0, 2, 1, 3).reshape(B, S, D) @ wo

    a = _A_ref(A_u, A_A_re, A_A_im, A_B_re, A_B_im, A_C_re, A_C_im, A_log_dt, A_Dskip)
    b = _B_ref(B_x, B_wqkv, B_wo)
    return torch.concatenate([a, b], axis=-1)
```

```cpp
#include <hip/hip_runtime.h>
#include <hip/hip_bf16.h>
typedef __hip_bfloat16 bf16;
static __device__ __forceinline__ float b2f(bf16 x){ return __bfloat162float(x);}
static __device__ __forceinline__ float rbf(float x){ return __bfloat162float(__float2bfloat16(x));}

__global__ void gemm_naive(const bf16* A, const bf16* B, bf16* C, int M, int N, int K, int ldc, int coff) {
  int n = blockIdx.x * 64 + threadIdx.x; int m = blockIdx.y;
  float s = 0;
  for (int k = 0; k < K; ++k) s += b2f(A[m*K+k]) * b2f(B[k*N+n]);
  C[m*ldc + coff + n] = __float2bfloat16(s);
}
__global__ void attn_naive(const bf16* qkv, bf16* o) {
  int q = blockIdx.x * 64 + threadIdx.x; int h = blockIdx.y;
  float qv[16]; for (int d=0; d<16; ++d) qv[d] = b2f(qkv[q*1536 + h*16 + d]);
  float m = -1e30f;
  for (int k=0;k<2048;++k){ float s=0; for(int d=0;d<16;++d) s+=qv[d]*b2f(qkv[k*1536+512+h*16+d]); s=rbf(s)*0.25f; m=fmaxf(m,s);}
  float l=0;
  for (int k=0;k<2048;++k){ float s=0; for(int d=0;d<16;++d) s+=qv[d]*b2f(qkv[k*1536+512+h*16+d]); s=rbf(s)*0.25f; l+=__expf(s-m);}
  float acc[16]; for(int d=0;d<16;++d) acc[d]=0;
  for (int k=0;k<2048;++k){ float s=0; for(int d=0;d<16;++d) s+=qv[d]*b2f(qkv[k*1536+512+h*16+d]); s=rbf(s)*0.25f; float p=rbf(__expf(s-m)/l);
    for(int d=0;d<16;++d) acc[d]+=p*b2f(qkv[k*1536+1024+h*16+d]); }
  for(int d=0;d<16;++d) o[q*512+h*16+d]=__float2bfloat16(acc[d]);
}
__global__ void kgen_naive(const bf16* Are,const bf16* Aim,const bf16* Bre,const bf16* Bim,const bf16* Cre,const bf16* Cim,const bf16* logdt, float* K){
  int l = blockIdx.x*64+threadIdx.x; int d = blockIdx.y;
  float dt = rbf(expf(b2f(logdt[d])));
  float lf = rbf((float)l);
  float s=0;
  for(int n=0;n<128;++n){
    float a=dt*b2f(Are[d*128+n]), b=dt*b2f(Aim[d*128+n]);
    float br=dt*b2f(Bre[d*128+n]), bi=dt*b2f(Bim[d*128+n]);
    float cr=b2f(Cre[d*128+n]), ci=b2f(Cim[d*128+n]);
    float e=expf(a*lf); float sn,cs; sincosf(b*lf,&sn,&cs);
    float pr=e*cs, pi=e*sn;
    float xr=cr*br-ci*bi, xi=cr*bi+ci*br;
    s+=xr*pr-xi*pi;
  }
  K[d*2048+l]=s;
}
__global__ void conv_naive(const bf16* u, const float* K, const bf16* Dsk, bf16* out){
  int d = blockIdx.x*64+threadIdx.x; int t = blockIdx.y;
  float s=0;
  for(int l=0;l<=t;++l) s+=K[d*2048+l]*b2f(u[(t-l)*512+d]);
  float y=rbf(s); float sk=rbf(b2f(u[t*512+d])*b2f(Dsk[d]));
  out[t*1024+d]=__float2bfloat16(y+sk);
}
extern "C" void kernel_launch(void* const* d_in, const int* in_sizes, int n_in, void* d_out, int out_size, void* d_ws, size_t ws_size, hipStream_t stream) {
  const bf16* u=(const bf16*)d_in[0];
  bf16* out=(bf16*)d_out;
  bf16* qkv=(bf16*)d_ws; bf16* o=qkv+2048*1536; float* K=(float*)(o+2048*512);
  gemm_naive<<<dim3(1536/64,2048),64,0,stream>>>((const bf16*)d_in[9],(const bf16*)d_in[10],qkv,2048,1536,512,1536,0);
  attn_naive<<<dim3(2048/64,32),64,0,stream>>>(qkv,o);
  gemm_naive<<<dim3(512/64,2048),64,0,stream>>>(o,(const bf16*)d_in[11],out,2048,512,512,1024,512);
  kgen_naive<<<dim3(2048/64,512),64,0,stream>>>((const bf16*)d_in[1],(const bf16*)d_in[2],(const bf16*)d_in[3],(const bf16*)d_in[4],(const bf16*)d_in[5],(const bf16*)d_in[6],(const bf16*)d_in[7],K);
  conv_naive<<<dim3(512/64,2048),64,0,stream>>>(u,K,(const bf16*)d_in[8],out);
}
```

```cpp
#include <hip/hip_runtime.h>

typedef unsigned short u16;
typedef unsigned long long u64;
typedef __attribute__((ext_vector_type(2))) unsigned u32x2;
typedef __attribute__((ext_vector_type(4))) unsigned u32x4;
typedef __attribute__((ext_vector_type(2))) float f32x2;
typedef __attribute__((ext_vector_type(4))) float f32x4;
typedef __attribute__((ext_vector_type(8))) float f32x8;
typedef __attribute__((ext_vector_type(16))) float f32x16;
typedef __attribute__((ext_vector_type(2))) __bf16 bf16x2;
typedef __attribute__((ext_vector_type(8))) __bf16 bf16x8;
typedef __attribute__((ext_vector_type(16))) __bf16 bf16x16;
typedef __attribute__((ext_vector_type(4))) short s16x4;
typedef __amdgpu_buffer_rsrc_t rsrc_t;
typedef __attribute__((address_space(1))) u64 gu64;
#define AS3 __attribute__((address_space(3)))
#define DEV static __device__ __forceinline__

constexpr unsigned WS_KV = 0;
constexpr unsigned WS_O = 4u << 20;
constexpr unsigned WS_UT = 6u << 20;
constexpr unsigned WS_F = 8u << 20;
constexpr unsigned WS_EPOCH = WS_F;
constexpr unsigned WS_UFLAG = WS_F + 4096;
constexpr unsigned WS_KVFLAG = WS_F + 8192;
constexpr unsigned WS_OFLAG = WS_F + 12288;
constexpr unsigned WS_KMAX = WS_F + 16384;
constexpr unsigned WS_USED = WS_F + 32768;

constexpr unsigned XS = 0;
constexpr unsigned RING = 65536;
constexpr unsigned SS = 131072;
constexpr unsigned TBL = SS;
constexpr unsigned UST = SS + 17408;
constexpr unsigned CNT = SS + 29888;
constexpr unsigned C_M = CNT, C_KVD = CNT + 4, C_L = CNT + 8, C_UD = CNT + 12, C_RDY = CNT + 16;
constexpr unsigned UARR = RING;
constexpr unsigned QF = RING + 2 * 16384;
constexpr unsigned PART = RING + 3 * 16384;
constexpr unsigned YB = RING + 16384;
constexpr unsigned OCB = RING;
constexpr unsigned ORB = RING + 32768;

#define NM_STEPS 32

DEV rsrc_t mkrs(const void* p, unsigned bytes) { return __builtin_amdgcn_make_buffer_rsrc((void*)p, (short)0, (int)bytes, 0x00020000); }
DEV unsigned rfl(unsigned x) { return __builtin_amdgcn_readfirstlane(x); }
DEV unsigned pk2(float a, float b) { f32x2 v = {a, b}; return __builtin_bit_cast(unsigned, __builtin_convertvector(v, bf16x2)); }
DEV float lo16f(unsigned x) { return __builtin_bit_cast(float, x << 16); }
DEV float hi16f(unsigned x) { return __builtin_bit_cast(float, x & 0xffff0000u); }
DEV float rbf(float a) { return lo16f(pk2(a, a)); }
template <typename T> DEV T ldsr(unsigned a) { return *(const AS3 T*)a; }
template <typename T> DEV void ldsw(unsigned a, T v) { *(AS3 T*)a = v; }
DEV u32x2 ldtr(unsigned a) { return __builtin_bit_cast(u32x2, __builtin_amdgcn_ds_read_tr16_b64_v4i16((AS3 s16x4*)a)); }
DEV bf16x8 asb8(u32x4 v) { return __builtin_bit_cast(bf16x8, v); }
DEV u32x4 cat(u32x2 a, u32x2 b) { return u32x4{a[0], a[1], b[0], b[1]}; }
DEV f32x16 mfma32(u32x4 a, u32x4 b, f32x16 c) { return __builtin_amdgcn_mfma_f32_32x32x16_bf16(asb8(a), asb8(b), c, 0, 0, 0); }
DEV f32x4 mfma16(u32x4 a, u32x4 b, f32x4 c) { return __builtin_amdgcn_mfma_f32_16x16x32_bf16(asb8(a), asb8(b), c, 0, 0, 0); }

#define GLDS(lds, voff, srd, soff) \
  asm volatile("s_mov_b32 m0, %0\n\ts_nop 0\n\tbuffer_load_dwordx4 %1, %2, %3 offen lds" ::"s"(lds), "v"(voff), "s"(srd), "s"(soff) : "memory")
#define VMW(n) asm volatile("s_waitcnt vmcnt(%0)" ::"n"(n) : "memory")
#define LGW0() asm volatile("s_waitcnt lgkmcnt(0)" ::: "memory")
#define BAR() do { asm volatile("s_waitcnt lgkmcnt(0)\n\ts_barrier" ::: "memory"); } while (0)

DEV void arrive(unsigned addr, int lane) {
  LGW0();
  if (lane == 0) __hip_atomic_fetch_add((AS3 unsigned*)addr, 1u, __ATOMIC_RELAXED, __HIP_MEMORY_SCOPE_WORKGROUP);
  asm volatile("" ::: "memory");
}
DEV unsigned arrive_ret(unsigned addr, int lane) {
  LGW0();
  unsigned r = 0;
  if (lane == 0) r = __hip_atomic_fetch_add((AS3 unsigned*)addr, 1u, __ATOMIC_RELAXED, __HIP_MEMORY_SCOPE_WORKGROUP);
  asm volatile("" ::: "memory");
  return rfl(r);
}
DEV void waitge(unsigned addr, unsigned target) {
  for (;;) {
    unsigned v;
    asm volatile("ds_read_b32 %0, %1\n\ts_waitcnt lgkmcnt(0)" : "=v"(v) : "v"(addr) : "memory");
    if (rfl(v) >= target) break;
  }
}
DEV void gsync(unsigned addr, unsigned target, int lane) { arrive(addr, lane); waitge(addr, target); }
DEV void pollflags(const char* p, int n, u64 tag, int lane) {
  gu64* f = (gu64*)(p) + (lane < n ? lane : 0);
  for (unsigned it = 0; it < (1u << 20); ++it) {
    u64 v = __hip_atomic_load(f, __ATOMIC_RELAXED, __HIP_MEMORY_SCOPE_AGENT);
    if (__builtin_amdgcn_ballot_w64(v != tag) == 0) break;
  }
  asm volatile("" ::: "memory");
}
DEV void setflag(char* p, u64 tag) { __hip_atomic_store((gu64*)p, tag, __ATOMIC_RELAXED, __HIP_MEMORY_SCOPE_AGENT); }
DEV u64 loadtag(const char* ws) {
  u64 e;
  asm volatile("s_load_dwordx2 %0, %1, 0x0\n\ts_waitcnt lgkmcnt(0)" : "=s"(e) : "s"(ws + WS_EPOCH) : "memory");
  return e * 6364136223846793005ull + 1442695040888963407ull;
}
DEV void st16sc1(u32x4 v, rsrc_t rs, unsigned voff, unsigned soff) {
  asm volatile("buffer_store_dwordx4 %0, %1, %2, %3 offen sc1\n\ts_nop 1" ::"v"(v), "v"(voff), "s"(rs), "s"(soff) : "memory");
}
DEV void st16(u32x4 v, rsrc_t rs, unsigned voff, unsigned soff) {
  asm volatile("buffer_store_dwordx4 %0, %1, %2, %3 offen\n\ts_nop 1" ::"v"(v), "v"(voff), "s"(rs), "s"(soff) : "memory");
}
DEV float xor32(float v) { return __shfl_xor(v, 32); }

typedef f32x2 cf;
DEV cf cmul(cf a, cf b) {
  cf t, r;
  asm("v_pk_mul_f32 %0, %1, %2 op_sel:[0,0] op_sel_hi:[0,1]" : "=v"(t) : "v"(a), "v"(b));
  asm("v_pk_fma_f32 %0, %1, %2, %3 op_sel:[1,1,0] op_sel_hi:[1,0,1] neg_lo:[0,1,0]" : "=v"(r) : "v"(a), "v"(b), "v"(t));
  return r;
}
DEV cf cexpl(float a2, float brev, float l) {
  float e = __builtin_amdgcn_exp2f(a2 * l);
  float ph = __builtin_amdgcn_fractf(brev * l);
  return cf{e * __builtin_amdgcn_cosf(ph), e * __builtin_amdgcn_sinf(ph)};
}

#define ST(k)

#define KVLOAD(b)                                                                                                   \
  do {                                                                                                              \
    asm volatile("buffer_load_dwordx4 %0, %3, %5, %6 offen sc1\n\ts_mov_b64 exec, %7\n\t"                          \
                 "buffer_load_dwordx4 %1, %4, %5, %6 offen offset:1024 sc1\n\t"                                     \
                 "buffer_load_dwordx4 %2, %4, %5, %6 offen offset:1536 sc1\n\ts_mov_b64 exec, -1"                   \
                 : "+v"(kf[b]), "+v"(va[b]), "+v"(vb[b])                                                            \
                 : "v"(kvo), "v"(vvo), "s"(rws), "s"(so), "s"(lomask)                                               \
                 : "memory");                                                                                       \
    so += 2048; so = (so == soe) ? sob : so;                                                                        \
  } while (0)
#define KVWAIT(n, b) asm volatile("s_waitcnt vmcnt(%3)" : "+v"(kf[b]), "+v"(va[b]), "+v"(vb[b]) : "n"(n) : "memory")

DEV void expv(f32x16& acc, f32x16 s, u32x4 va, u32x4 vb) {
  f32x8 p0, p1;
#pragma unroll
  for (int j = 0; j < 8; ++j) p0[j] = __builtin_amdgcn_exp2f(s[j]);
  acc = __builtin_amdgcn_smfmac_f32_32x32x32_bf16(asb8(va), __builtin_bit_cast(bf16x16, p0), acc, (int)0xDDDDDDDD, 0, 0);
#pragma unroll
  for (int j = 0; j < 8; ++j) p1[j] = __builtin_amdgcn_exp2f(s[8 + j]);
  acc = __builtin_amdgcn_smfmac_f32_32x32x32_bf16(asb8(vb), __builtin_bit_cast(bf16x16, p1), acc, (int)0xDDDDDDDD, 0, 0);
}
DEV float sq8(u32x4 q) {
  float s = 0.f;
#pragma unroll
  for (int j = 0; j < 4; ++j) { float a = lo16f(q[j]), b = hi16f(q[j]); s += a * a; s += b * b; }
  return s;
}

__global__ void __launch_bounds__(512, 2)
fused_kernel(const u16* __restrict__ pu, const u16* __restrict__ pAre, const u16* __restrict__ pAim, const u16* __restrict__ pBre,
             const u16* __restrict__ pBim, const u16* __restrict__ pCre, const u16* __restrict__ pCim, const u16* __restrict__ pLdt,
             const u16* __restrict__ pDsk, const u16* __restrict__ px, const u16* __restrict__ pW, const u16* __restrict__ pWo,
             u16* __restrict__ pout, char* __restrict__ ws) {
  __shared__ __attribute__((aligned(16))) char smem[163840];
  asm volatile("" ::"s"((unsigned)(size_t)(AS3 char*)smem));
  ST(0);
  const int tid = threadIdx.x, lane = tid & 63;
  const unsigned w = rfl(tid >> 6);
  const unsigned hg = blockIdx.x & 7, rt = blockIdx.x >> 3;
  const unsigned r = lane & 31, hh = lane >> 5;
  const rsrc_t rws = mkrs(ws, WS_USED);
  const rsrc_t rout = mkrs(pout, 2048 * 1024 * 2);
  u64 tag;
  u32x4 kf[4], va[4], vb[4];
  unsigned so, sob, soe;
  const u64 lomask = 0x0000FFFF0000FFFFull;
  const unsigned kvo = lane * 16, vvo = hh * 256 + (lane & 15) * 16;
  const unsigned hl = w & 3, head = hg * 4 + hl;
  float kmx;
  {
    const unsigned n = w < 4 ? NM_STEPS : 64 - NM_STEPS, t0 = w < 4 ? 0 : NM_STEPS;
    sob = WS_KV + (head * 64 + t0) * 2048; soe = sob + n * 2048;
    so = sob + ((rt * n) >> 5) * 2048;
  }
#pragma unroll
  for (int b = 0; b < 4; ++b) { kf[b] = u32x4{0, 0, 0, 0}; va[b] = u32x4{0x3f803f80u, 0x3f803f80u, 0x3f803f80u, 0x3f803f80u}; vb[b] = va[b]; }

  if (w < 4) {
    const rsrc_t rx = mkrs(px, 2048 * 512 * 2), rw = mkrs(pW, 512 * 1536 * 2);
    const unsigned rr = lane >> 3, c8 = lane & 7;
    const unsigned xo0 = (64 * rt + 16 * w + rr) * 1024 + ((c8 ^ (rr >> 1)) << 4);
    const unsigned xo1 = (64 * rt + 16 * w + 8 + rr) * 1024 + ((c8 ^ (4 + (rr >> 1))) << 4);
    const unsigned wof = (16 * w + rr) * 3072 + hg * 128 + ((c8 ^ (((rr >> 1) & 1) << 2)) << 4);
    const unsigned ld0 = w * 2048;
#define SLAB(i) (((i) + rt) & 7)
#define LDX(i) do { GLDS(XS + (i) * 8192 + ld0, xo0, rx, SLAB(i) * 128); GLDS(XS + (i) * 8192 + ld0 + 1024, xo1, rx, SLAB(i) * 128); } while (0)
#define LDW(dst, i, part) do { GLDS((dst) + ld0, wof, rw, SLAB(i) * 196608 + (part) * 1024); GLDS((dst) + ld0 + 1024, wof, rw, SLAB(i) * 196608 + (part) * 1024 + 24576); } while (0)
#define LDKV(i) do { LDW(RING + ((i) & 3) * 16384, i, 1); LDW(RING + ((i) & 3) * 16384 + 8192, i, 2); } while (0)
#define LDQ(slot, i) do { LDW(RING + (slot) * 16384, i, 0); LDW(RING + (slot) * 16384 + 8192, (i) + 1, 0); } while (0)
    LDX(0); LDKV(0); LDX(1); LDKV(1); LDX(2); LDKV(2); LDX(3); LDKV(3);
    LDX(4); LDX(5); LDX(6); LDX(7);
    tag = loadtag(ws);
    ST(1);
    BAR();
    const unsigned th = w & 1, p = w >> 1;
    unsigned xa[4];
#pragma unroll
    for (int ks = 0; ks < 4; ++ks) xa[ks] = XS + (32 * th + r) * 128 + (((2 * ks + hh) ^ ((r >> 1) & 7)) << 4);
    const unsigned q4 = (lane & 15) >> 2;
    const unsigned wa = RING + (8 * hh + q4) * 128 + ((p * 64 + ((lane >> 4) & 1) * 32 + (lane & 3) * 8) ^ (((q4 >> 1) & 1) << 6));
    f32x16 accK = {0}, accV = {0};
#define KVSLAB(i)                                                                                     \
  _Pragma("unroll") for (int ks = 0; ks < 4; ++ks) {                                                  \
    u32x4 xf = ldsr<u32x4>(xa[ks] + (i) * 8192);                                                      \
    unsigned a = wa + ((i) & 3) * 16384 + ks * 2048;                                                  \
    u32x4 wk = cat(ldtr(a), ldtr(a + 512)), wv = cat(ldtr(a + 8192), ldtr(a + 8192 + 512));           \
    accK = mfma32(wk, xf, accK); accV = mfma32(xf, wv, accV);                                         \
  }
    VMW(26); gsync(C_M, 4, lane); ST(2); KVSLAB(0);
    VMW(20); gsync(C_M, 8, lane); LDKV(4); KVSLAB(1);
    VMW(18); gsync(C_M, 12, lane); LDKV(5); KVSLAB(2);
    VMW(16); gsync(C_M, 16, lane); LDKV(6); KVSLAB(3);
    VMW(8); gsync(C_M, 20, lane); ST(3); LDKV(7); KVSLAB(4);
    VMW(8); gsync(C_M, 24, lane); KVSLAB(5);
    VMW(4); gsync(C_M, 28, lane); LDQ(1, 0); KVSLAB(6);
    VMW(4); gsync(C_M, 32, lane); LDQ(2, 2); KVSLAB(7);
    ST(4);
    {
      const unsigned tile = 2 * rt + th;
      const unsigned rsw = (r & 0x13) | ((r & 4) << 1) | ((r & 8) >> 1);
      const unsigned kb = WS_KV + ((hg * 4 + 2 * p) * 64 + tile) * 2048;
      u32x4 k0 = {pk2(accK[0], accK[1]), pk2(accK[2], accK[3]), pk2(accK[4], accK[5]), pk2(accK[6], accK[7])};
      u32x4 k1 = {pk2(accK[8], accK[9]), pk2(accK[10], accK[11]), pk2(accK[12], accK[13]), pk2(accK[14], accK[15])};
      u32x4 v0 = {pk2(accV[0], accV[1]), pk2(accV[2], accV[3]), pk2(accV[4], accV[5]), pk2(accV[6], accV[7])};
      u32x4 v1 = {pk2(accV[8], accV[9]), pk2(accV[10], accV[11]), pk2(accV[12], accV[13]), pk2(accV[14], accV[15])};
      st16sc1(k0, rws, (hh * 32 + rsw) * 16, kb);
      st16sc1(k1, rws, (hh * 32 + rsw) * 16, kb + 131072);
      const unsigned vo = (r >> 4) * 131072 + 1024 + vvo;
      st16sc1(v0, rws, vo, kb);
      st16sc1(v1, rws, vo + 512, kb);
      float n0 = sq8(k0), n1 = sq8(k1);
      n0 += xor32(n0); n1 += xor32(n1);
      float m = hh ? n1 : n0;
#pragma unroll
      for (int o = 16; o >= 1; o >>= 1) m = fmaxf(m, __shfl_xor(m, o));
      __builtin_amdgcn_raw_buffer_store_b32(__builtin_bit_cast(unsigned, m), rws, hh * 256, WS_KMAX + ((hg * 4 + 2 * p) * 64 + tile) * 4, 16);
    }
    asm volatile("" ::: "memory");
    f32x16 accQ = {0};
#define QSLAB(i, base)                                                                                \
  _Pragma("unroll") for (int ks = 0; ks < 4; ++ks) {                                                  \
    u32x4 xf = ldsr<u32x4>(xa[ks] + (i) * 8192);                                                      \
    unsigned a = wa + (base) + ks * 2048;                                                             \
    u32x4 wq = cat(ldtr(a), ldtr(a + 512));                                                           \
    accQ = mfma32(wq, xf, accQ);                                                                      \
  }
    VMW(9); gsync(C_M, 36, lane); LDQ(3, 4); QSLAB(0, 16384); QSLAB(1, 16384 + 8192);
    VMW(9); gsync(C_M, 40, lane); LDQ(1, 6); QSLAB(2, 32768); QSLAB(3, 32768 + 8192);
    ST(5);
    VMW(8);
    if (arrive_ret(C_KVD, lane) == 3 && lane == 0) setflag(ws + WS_KVFLAG + (hg * 32 + rt) * 8, tag);
    ST(6);
    VMW(0); gsync(C_M, 44, lane);
    QSLAB(4, 49152); QSLAB(5, 49152 + 8192); QSLAB(6, 16384); QSLAB(7, 16384 + 8192);
    {
      const float qs = 0.25f * 1.44269504088896341f;
      u32x4 q0 = {pk2(accQ[0] * qs, accQ[1] * qs), pk2(accQ[2] * qs, accQ[3] * qs), pk2(accQ[4] * qs, accQ[5] * qs), pk2(accQ[6] * qs, accQ[7] * qs)};
      u32x4 q1 = {pk2(accQ[8] * qs, accQ[9] * qs), pk2(accQ[10] * qs, accQ[11] * qs), pk2(accQ[12] * qs, accQ[13] * qs), pk2(accQ[14] * qs, accQ[15] * qs)};
      ldsw<u32x4>(QF + ((2 * p) * 2 + th) * 1024 + lane * 16, q0);
      ldsw<u32x4>(QF + ((2 * p + 1) * 2 + th) * 1024 + lane * 16, q1);
    }
    arrive(C_M, lane);
    ST(7);
    pollflags(ws + WS_KVFLAG + hg * 256, 32, tag, lane);
    arrive(C_RDY, lane);
    ST(8);
    asm volatile("buffer_load_dword %0, %1, %2, %3 offen sc1" : "=v"(kmx) : "v"(lane * 4), "s"(rws), "s"(WS_KMAX + head * 256) : "memory");
    KVLOAD(0); KVLOAD(1); KVLOAD(2); KVLOAD(3);
    waitge(C_M, 48);
    asm volatile("s_waitcnt vmcnt(0)" : "+v"(kmx)::"memory");
    {
      const rsrc_t rwo = mkrs(pWo, 512 * 512 * 2);
      const unsigned oo = (128 * w + rr) * 1024 + hg * 128 + ((c8 ^ (((rr >> 1) & 1) << 2)) << 4);
#pragma unroll
      for (int j = 0; j < 16; ++j) GLDS(XS + w * 16384 + j * 1024, oo, rwo, j * 8192);
    }
  } else {
    const unsigned v = w - 4, cc = v >> 1, a = v & 1;
    const unsigned d = 64 * hg + 2 * rt + cc;
    const rsrc_t ru = mkrs(pu, 2048 * 512 * 2);
    {
      const unsigned uo = (64 * rt + 16 * v + (lane >> 3)) * 1024 + hg * 128 + (lane & 7) * 16;
      GLDS(UST + v * 2048, uo, ru, 0u); GLDS(UST + v * 2048 + 1024, uo, ru, 8192u);
    }
    unsigned pa[6], pl, pd;
    {
      const unsigned po = (d * 128 + 64 * a + lane) * 2, pdo = d * 2;
      asm volatile("global_load_ushort %0, %1, %2" : "=v"(pa[0]) : "v"(po), "s"(pAre) : "memory");
      asm volatile("global_load_ushort %0, %1, %2" : "=v"(pa[1]) : "v"(po), "s"(pAim) : "memory");
      asm volatile("global_load_ushort %0, %1, %2" : "=v"(pa[2]) : "v"(po), "s"(pBre) : "memory");
      asm volatile("global_load_ushort %0, %1, %2" : "=v"(pa[3]) : "v"(po), "s"(pBim) : "memory");
      asm volatile("global_load_ushort %0, %1, %2" : "=v"(pa[4]) : "v"(po), "s"(pCre) : "memory");
      asm volatile("global_load_ushort %0, %1, %2" : "=v"(pa[5]) : "v"(po), "s"(pCim) : "memory");
      asm volatile("global_load_ushort %0, %1, %2" : "=v"(pl) : "v"(pdo), "s"(pLdt) : "memory");
      asm volatile("global_load_ushort %0, %1, %2" : "=v"(pd) : "v"(pdo), "s"(pDsk) : "memory");
    }
    if (v == 0 && lane < 16) ldsw<unsigned>(CNT + lane * 4, 0u);
    tag = loadtag(ws);
    ST(1);
    BAR();
    VMW(8); gsync(C_L, 4, lane);
    ST(2);
    {
      const unsigned g = lane >> 4, ta = UST + (8 * g + ((lane & 15) >> 2)) * 128 + v * 32 + (lane & 3) * 8;
      u32x4 t0 = cat(ldtr(ta), ldtr(ta + 512)), t1 = cat(ldtr(ta + 4096), ldtr(ta + 4096 + 512));
      const unsigned go = (64 * hg + 16 * v + (lane & 15)) * 4096 + rt * 128 + g * 16;
      st16sc1(t0, rws, go, WS_UT);
      st16sc1(t1, rws, go + 64, WS_UT);
    }
    asm volatile("s_waitcnt vmcnt(2)" : "+v"(pa[0]), "+v"(pa[1]), "+v"(pa[2]), "+v"(pa[3]), "+v"(pa[4]), "+v"(pa[5]), "+v"(pl), "+v"(pd)::"memory");
    const float dsk = lo16f(pd);
    {
      const float dt = rbf(expf(lo16f(pl)));
      const float a2 = dt * lo16f(pa[0]) * 1.44269504088896341f, br = dt * lo16f(pa[1]) * 0.15915494309189535f;
      const cf c = cmul(cf{lo16f(pa[4]), -lo16f(pa[5])}, cf{dt * lo16f(pa[2]), -dt * lo16f(pa[3])});
      const unsigned tw = TBL + v * 4352 + lane * 4;
      const unsigned tr = TBL + v * 4352 + (lane & 15) * 272 + (lane >> 4) * 16;
      const cf e1 = cexpl(a2, -br, 1.f), e256 = cexpl(a2, -br, 256.f), e8 = cexpl(a2, br, 8.f), e64 = cexpl(a2, br, 64.f);
      const cf e2 = cmul(e1, e1), e4 = cmul(e2, e2);
#define PUTG(row, g) do { cf g_ = (g); ldsw<unsigned>(tw + (row) * 272, pk2(g_[0], g_[1])); } while (0)
#define PUTE(row, g) PUTG(row, g)
      u32x4 h0[4], h1[4];
      cf g256 = cmul(c, e256), g512 = cmul(g256, e256), g768 = cmul(g512, e256);
      {
        cf g1 = cmul(c, e1), g2 = cmul(c, e2), g4 = cmul(c, e4);
        cf g3 = cmul(g2, e1), g5 = cmul(g4, e1), g6 = cmul(g4, e2), g7 = cmul(g3, e4);
        PUTG(0, c); PUTG(1, g1); PUTG(2, g2); PUTG(3, g3); PUTG(4, g4); PUTG(5, g5); PUTG(6, g6); PUTG(7, g7);
        cf g258 = cmul(g256, e2), g260 = cmul(g256, e4), g262 = cmul(g258, e4);
        PUTG(8, g256); PUTG(9, g258); PUTG(10, g260); PUTG(11, g262);
        PUTG(12, g512); PUTG(13, cmul(g512, e4)); PUTG(14, g768); PUTG(15, cmul(g768, e4));
      }
#pragma unroll
      for (int ks = 0; ks < 4; ++ks) h0[ks] = ldsr<u32x4>(tr + ks * 64);
      asm volatile("" ::: "memory");
      {
        cf g = g768;
#pragma unroll
        for (int i = 0; i < 5; ++i) { g = cmul(g, e256); PUTG(i, g); }
      }
#pragma unroll
      for (int ks = 0; ks < 4; ++ks) h1[ks] = ldsr<u32x4>(tr + ks * 64);
      asm volatile("" ::: "memory");
      f32x4 dd[2][2];
      cf s0 = cf{1.f, 0.f}, s1 = e64;
      const cf e128 = cmul(e64, e64);
#pragma unroll
      for (int lt = 0; lt < 2; ++lt) {
        cf x0 = s0, x1 = s1;
#pragma unroll
        for (int i = 0; i < 8; ++i) {
          PUTE(i, x0); PUTE(8 + i, x1);
          if (i < 7) { x0 = cmul(x0, e8); x1 = cmul(x1, e8); }
        }
        u32x4 lf[4];
#pragma unroll
        for (int ks = 0; ks < 4; ++ks) lf[ks] = ldsr<u32x4>(tr + ks * 64);
        asm volatile(
            "v_mfma_f32_16x16x32_bf16 %0, %2, %6, 0\n\tv_mfma_f32_16x16x32_bf16 %1, %2, %10, 0\n\t"
            "v_mfma_f32_16x16x32_bf16 %0, %3, %7, %0\n\tv_mfma_f32_16x16x32_bf16 %1, %3, %11, %1\n\t"
            "v_mfma_f32_16x16x32_bf16 %0, %4, %8, %0\n\tv_mfma_f32_16x16x32_bf16 %1, %4, %12, %1\n\t"
            "v_mfma_f32_16x16x32_bf16 %0, %5, %9, %0\n\tv_mfma_f32_16x16x32_bf16 %1, %5, %13, %1\n\ts_nop 7"
            : "=&v"(dd[lt][0]), "=&v"(dd[lt][1])
            : "v"(lf[0]), "v"(lf[1]), "v"(lf[2]), "v"(lf[3]), "v"(h0[0]), "v"(h0[1]), "v"(h0[2]), "v"(h0[3]), "v"(h1[0]), "v"(h1[1]),
              "v"(h1[2]), "v"(h1[3])
            : "memory");
        s0 = e128; s1 = cmul(e128, e64);
      }
      const unsigned pw = TBL + v * 4352 + (lane & 15) * 144 + (lane >> 4) * 16;
      ldsw<f32x4>(pw, dd[0][0]); ldsw<f32x4>(pw + 64, dd[1][0]);
      if ((lane & 15) < 5) { ldsw<f32x4>(pw + 16 * 144, dd[0][1]); ldsw<f32x4>(pw + 16 * 144 + 64, dd[1][1]); }
    }
    ST(3);
    VMW(0);
    if (arrive_ret(C_UD, lane) == 3 && lane == 0) setflag(ws + WS_UFLAG + (hg * 32 + rt) * 8, tag);
    gsync(C_L, 8, lane);
    const unsigned cb0 = cc ? SS + 21568 : SS + 17408, cb1 = cc ? SS + 0 : SS + 4160, cb2 = cc ? SS + 25728 : SS + 8320,
                   cb3 = cc ? RING + 10432 : SS + 12480;
    u32x4 dk[2];
    {
      const unsigned P = TBL + cc * 8704, lo = lane & 31;
#define RDP(ad) (ldsr<float>(ad) + ldsr<float>((ad) + 4352))
      if (a == 0) {
        const unsigned b0 = P + lo * 4 + (hh ? 8 * 144 : 0);
        float t[8];
        t[0] = RDP(b0);
        t[1] = RDP(b0 + (hh ? 0 : 144));
        t[2] = RDP(b0 + (hh ? 144 : 2 * 144));
        t[3] = RDP(b0 + (hh ? 2 * 144 : 3 * 144));
        t[4] = RDP(b0 + (hh ? 2 * 144 : 4 * 144));
        t[5] = RDP(b0 + (hh ? 2 * 144 : 5 * 144));
        t[6] = RDP(b0 + (hh ? 3 * 144 : 6 * 144));
        t[7] = RDP(hh ? (lo < 31 ? b0 + 4 : P + 12 * 144) : b0 + 7 * 144);
        dk[0] = u32x4{pk2(t[7], t[6]), pk2(t[5], t[4]), pk2(t[3], t[2]), pk2(t[1], t[0])};
        const unsigned b1 = P + (12 + 2 * hh) * 144 + lo * 4;
        float v0 = RDP(b1), v4 = RDP(b1 + 144), v8 = RDP(lo < 31 ? b1 + 4 : P + (14 + 2 * hh) * 144);
        dk[1] = u32x4{pk2(v8, v8), pk2(v4, v4), pk2(v4, v0), pk2(v0, v0)};
      } else {
#pragma unroll
        for (int k = 0; k < 2; ++k) {
          const unsigned b1 = P + (16 + 2 * k + hh) * 144 + lo * 4;
          float v0 = RDP(b1), v8 = RDP(lo < 31 ? b1 + 4 : P + (17 + 2 * k + hh) * 144);
          dk[k] = u32x4{pk2(v8, v8), pk2(v8, (lane & 1) ? v8 : v0), pk2(v0, v0), pk2(v0, v0)};
        }
      }
    }
    const unsigned zo = (255 - 128 * a - lane) * 16;
    const u32x4 z4 = {0, 0, 0, 0};
    ldsw<u32x4>(cb0 + zo, dk[0]); ldsw<u32x4>(cb0 + zo - 1024, dk[1]);
    if (a == 1 && lane < 4) ldsw<u32x4>(cb0 + 4096 + lane * 16, z4);
    gsync(C_L, 12, lane);
    waitge(C_M, 24);
#pragma unroll
    for (int k = 0; k < 2; ++k) {
      const unsigned o = zo - 1024 * k;
      u32x2 n = ldsr<u32x2>(cb0 + o + 16);
      unsigned e0 = __builtin_amdgcn_alignbit(dk[k][1], dk[k][0], 16), e1 = __builtin_amdgcn_alignbit(dk[k][2], dk[k][1], 16),
               e2 = __builtin_amdgcn_alignbit(dk[k][3], dk[k][2], 16), e3 = __builtin_amdgcn_alignbit(n[0], dk[k][3], 16),
               e4 = __builtin_amdgcn_alignbit(n[1], n[0], 16);
      ldsw<u32x4>(cb1 + o, u32x4{e0, e1, e2, e3});
      ldsw<u32x4>(cb2 + o, u32x4{dk[k][1], dk[k][2], dk[k][3], n[0]});
      ldsw<u32x4>(cb3 + o, u32x4{e1, e2, e3, e4});
    }
    const unsigned ua = UARR + cc * 5200;
    if (a == 1 && lane < 16) {
      const unsigned g = lane >> 2;
      ldsw<u32x4>((g == 0 ? cb1 + 4096 : g == 1 ? cb2 + 4096 : g == 2 ? cb3 + 4096 : ua) + (lane & 3) * 16, z4);
    }
    ST(4);
    pollflags(ws + WS_UFLAG + hg * 256, 32, tag, lane);
    ST(5);
    {
      u32x4 u0 = __builtin_amdgcn_raw_buffer_load_b128(rws, lane * 16, WS_UT + d * 4096 + a * 2048, 16);
      u32x4 u1 = __builtin_amdgcn_raw_buffer_load_b128(rws, lane * 16 + 1024, WS_UT + d * 4096 + a * 2048, 16);
      const unsigned uw = ua + (32 * a + (lane >> 2) + 1) * 80 + (lane & 3) * 16;
      ldsw<u32x4>(uw, u0); ldsw<u32x4>(uw + 16 * 80, u1);
    }
    gsync(C_L, 16, lane);
    ST(6);
    f32x16 d0 = {0}, d1 = {0};
    {
      const unsigned c = 3 - (r & 3);
      const unsigned cb = c == 0 ? cb0 : c == 1 ? cb1 : c == 2 ? cb2 : cb3;
      const unsigned ab = cb + 4088 + 16 * hh - 8 * (r >> 2) - 64 * (16 * a + 15) - 2048;
      const int ub = (int)(ua + 16 * hh), un = (int)(80 * (r + 1) - 80 * 16 * a);
      const unsigned uh = ua + 16 * hh + 80 * (r + 33 - 16 * a - 15);
#pragma unroll
      for (int mm = 0; mm < 16; ++mm) {
        const int t = un - 80 * mm;
        const unsigned ul = (unsigned)(ub + (t > 0 ? t : 0));
#pragma unroll
        for (int kk = 0; kk < 2; ++kk) {
          const unsigned o = ab + (15 - mm) * 64 + 32 * kk;
          u32x4 alo = cat(ldsr<u32x2>(o + 2048), ldsr<u32x2>(o + 2048 + 8));
          u32x4 ahi = cat(ldsr<u32x2>(o), ldsr<u32x2>(o + 8));
          u32x4 bl = ldsr<u32x4>(ul + 32 * kk), bh = ldsr<u32x4>(uh + (15 - mm) * 80 + 32 * kk);
          d0 = mfma32(alo, bl, d0); d1 = mfma32(alo, bh, d1); d1 = mfma32(ahi, bl, d1);
        }
      }
    }
    ST(7);
    waitge(C_RDY, 1);
    asm volatile("buffer_load_dword %0, %1, %2, %3 offen sc1" : "=v"(kmx) : "v"(lane * 4), "s"(rws), "s"(WS_KMAX + head * 256) : "memory");
    KVLOAD(0); KVLOAD(1); KVLOAD(2); KVLOAD(3);
    waitge(C_M, 48);
    ST(8);
    {
      const f32x16 snd = a ? d0 : d1;
      f32x16 kp = a ? d1 : d0;
#pragma unroll
      for (int j = 0; j < 4; ++j) ldsw<f32x4>(PART + v * 4096 + j * 1024 + lane * 16, f32x4{snd[4 * j], snd[4 * j + 1], snd[4 * j + 2], snd[4 * j + 3]});
      gsync(C_L, 20, lane);
      const unsigned yw = YB + (32 * a + r) * 132 + hh * 16 + cc * 2;
#pragma unroll
      for (int j = 0; j < 4; ++j) {
        f32x4 o = ldsr<f32x4>(PART + (v ^ 1) * 4096 + j * 1024 + lane * 16);
        u32x2 uu = ldsr<u32x2>(ua + (32 * a + r + 1) * 80 + j * 16 + hh * 8);
        float uf[4] = {lo16f(uu[0]), hi16f(uu[0]), lo16f(uu[1]), hi16f(uu[1])};
#pragma unroll
        for (int e = 0; e < 4; ++e) {
          unsigned ys = pk2(kp[4 * j + e] + o[e], uf[e] * dsk);
          float y = lo16f(ys) + hi16f(ys);
          ldsw<u16>(yw + j * 32 + e * 4, (u16)pk2(y, y));
        }
      }
      gsync(C_L, 24, lane);
      unsigned yv[8];
#pragma unroll
      for (int j = 0; j < 8; ++j) yv[j] = ldsr<unsigned>(YB + (16 * v + 2 * j + hh) * 132 + r * 4);
      asm volatile("s_waitcnt vmcnt(0)" : "+v"(kmx)::"memory");
#pragma unroll
      for (int j = 0; j < 8; ++j)
        __builtin_amdgcn_raw_buffer_store_b32(yv[j], rout, lane * 2048 + (64 * hg + 2 * rt) * 2, (512 * v + 64 * j) * 2048, 0);
      asm volatile("" ::: "memory");
    }
  }

  ST(9);
  f32x16 o0 = {0}, o1 = {0};
  {
    const u32x4 q0 = ldsr<u32x4>(QF + (hl * 2) * 1024 + lane * 16), q1 = ldsr<u32x4>(QF + (hl * 2 + 1) * 1024 + lane * 16);
#pragma unroll
    for (int o = 32; o >= 1; o >>= 1) kmx = fmaxf(kmx, __shfl_xor(kmx, o));
    float n0 = sq8(q0), n1 = sq8(q1);
    n0 += xor32(n0); n1 += xor32(n1);
    float m0 = __builtin_sqrtf(n0 * kmx) * 1.001f, m1 = __builtin_sqrtf(n1 * kmx) * 1.001f;
    if (__builtin_amdgcn_ballot_w64(!(m0 <= 40.f && m1 <= 40.f)) != 0) {
      m0 = -INFINITY; m1 = -INFINITY;
      const f32x16 z = {0};
      for (int t = 0; t < 64; ++t) {
        u32x4 k = __builtin_amdgcn_raw_buffer_load_b128(rws, kvo, WS_KV + (head * 64 + t) * 2048, 16);
        f32x16 s0 = mfma32(k, q0, z), s1 = mfma32(k, q1, z);
#pragma unroll
        for (int j = 0; j < 16; ++j) { m0 = fmaxf(m0, s0[j]); m1 = fmaxf(m1, s1[j]); }
      }
      m0 = fmaxf(m0, xor32(m0)); m1 = fmaxf(m1, xor32(m1));
    }
    f32x16 c0, c1;
#pragma unroll
    for (int j = 0; j < 16; ++j) { c0[j] = -m0; c1[j] = -m1; }
    asm volatile("" : "+v"(kf[0]), "+v"(va[0]), "+v"(vb[0]));
    f32x16 sa = mfma32(kf[0], q0, c0);
    const int nit = (w < 4 ? NM_STEPS : 64 - NM_STEPS) / 4;
    for (int it = 0; it < nit; ++it) {
#pragma unroll
      for (int b = 0; b < 4; ++b) {
        f32x16 sb = mfma32(kf[b], q1, c1);
        expv(o0, sa, va[b], vb[b]);
        KVWAIT(6, (b + 1) & 3);
        sa = mfma32(kf[(b + 1) & 3], q0, c0);
        expv(o1, sb, va[b], vb[b]);
        KVLOAD(b);
      }
    }
  }
  ST(10);
  VMW(0);
  {
    const unsigned kh = w >> 2;
    const f32x16 snd = kh ? o0 : o1, kp = kh ? o1 : o0;
    BAR();
    ldsw<f32x4>(OCB + w * 2304 + lane * 16, f32x4{snd[0], snd[1], snd[2], snd[3]});
    ldsw<f32x4>(OCB + w * 2304 + 1024 + lane * 16, f32x4{snd[4], snd[5], snd[6], snd[7]});
    ldsw<float>(OCB + w * 2304 + 2048 + lane * 4, snd[8]);
    BAR();
    const f32x4 a0 = ldsr<f32x4>(OCB + (w ^ 4) * 2304 + lane * 16), a1 = ldsr<f32x4>(OCB + (w ^ 4) * 2304 + 1024 + lane * 16);
    const float inv = 1.0f / (kp[8] + ldsr<float>(OCB + (w ^ 4) * 2304 + 2048 + lane * 4));
    u32x4 ov = {pk2((kp[0] + a0[0]) * inv, (kp[1] + a0[1]) * inv), pk2((kp[2] + a0[2]) * inv, (kp[3] + a0[3]) * inv),
                pk2((kp[4] + a1[0]) * inv, (kp[5] + a1[1]) * inv), pk2((kp[6] + a1[2]) * inv, (kp[7] + a1[3]) * inv)};
    st16sc1(ov, rws, lane * 16, WS_O + ((2 * rt + kh) * 32 + head) * 1024);
  }
  {
    const unsigned qt = w & 1, nt = (w >> 1) & 1, k2 = w >> 2;
    const unsigned q4 = (lane & 15) >> 2, pq = lane & 3;
    const unsigned wa = XS + (256 * k2 + 4 * hh + q4) * 128 + ((nt * 64 + 32 * (pq & 1) + 16 * ((lane >> 4) & 1) + 8 * (pq >> 1)) ^ (((q4 >> 1) & 1) << 6));
    u32x4 af[16];
#pragma unroll
    for (int j = 0; j < 16; ++j) af[j] = cat(ldtr(wa + j * 2048), ldtr(wa + j * 2048 + 1024));
    VMW(0);
    BAR();
    if (tid == 0) setflag(ws + WS_OFLAG + (rt * 8 + hg) * 8, tag);
    ST(11);
    pollflags(ws + WS_OFLAG + rt * 64, 8, tag, lane);
    ST(12);
    u32x4 of[16];
#pragma unroll
    for (int j = 0; j < 16; ++j) of[j] = __builtin_amdgcn_raw_buffer_load_b128(rws, lane * 16 + j * 1024, WS_O + ((2 * rt + qt) * 32 + 16 * k2) * 1024, 16);
    f32x16 acc = {0};
#pragma unroll
    for (int j = 0; j < 16; ++j) acc = mfma32(af[j], of[j], acc);
    ST(13);
    const int so8 = k2 ? 0 : 8, ko8 = k2 ? 8 : 0;
    f32x8 snd, kp;
#pragma unroll
    for (int j = 0; j < 8; ++j) { snd[j] = k2 ? acc[j] : acc[8 + j]; kp[j] = k2 ? acc[8 + j] : acc[j]; }
    (void)so8; (void)ko8;
    ldsw<f32x4>(ORB + w * 2048 + lane * 16, f32x4{snd[0], snd[1], snd[2], snd[3]});
    ldsw<f32x4>(ORB + w * 2048 + 1024 + lane * 16, f32x4{snd[4], snd[5], snd[6], snd[7]});
    BAR();
    const f32x4 a0 = ldsr<f32x4>(ORB + (w ^ 4) * 2048 + lane * 16), a1 = ldsr<f32x4>(ORB + (w ^ 4) * 2048 + 1024 + lane * 16);
    u32x4 ov = {pk2(kp[0] + a0[0], kp[1] + a0[1]), pk2(kp[2] + a0[2], kp[3] + a0[3]), pk2(kp[4] + a1[0], kp[5] + a1[1]), pk2(kp[6] + a1[2], kp[7] + a1[3])};
    st16(ov, rout, r * 2048 + hh * 32, (64 * rt + 32 * qt) * 2048 + 1024 + (64 * hg + 32 * nt + 8 * k2) * 2);
    if (blockIdx.x == 0 && tid == 0) setflag(ws + WS_EPOCH, tag);
  }
}

extern "C" void kernel_launch(void* const* d_in, const int* in_sizes, int n_in, void* d_out, int out_size, void* d_ws, size_t ws_size,
                              hipStream_t stream) {
  fused_kernel<<<256, 512, 0, stream>>>((const u16*)d_in[0], (const u16*)d_in[1], (const u16*)d_in[2], (const u16*)d_in[3],
                                        (const u16*)d_in[4], (const u16*)d_in[5], (const u16*)d_in[6], (const u16*)d_in[7],
                                        (const u16*)d_in[8], (const u16*)d_in[9], (const u16*)d_in[10], (const u16*)d_in[11],
                                        (u16*)d_out, (char*)d_ws);
}
```

```cpp
#include <hip/hip_runtime.h>

typedef unsigned short u16;
typedef unsigned long long u64;
typedef __attribute__((ext_vector_type(2))) unsigned u32x2;
typedef __attribute__((ext_vector_type(4))) unsigned u32x4;
typedef __attribute__((ext_vector_type(2))) float f32x2;
typedef __attribute__((ext_vector_type(4))) float f32x4;
typedef __attribute__((ext_vector_type(8))) float f32x8;
typedef __attribute__((ext_vector_type(16))) float f32x16;
typedef __attribute__((ext_vector_type(2))) __bf16 bf16x2;
typedef __attribute__((ext_vector_type(8))) __bf16 bf16x8;
typedef __attribute__((ext_vector_type(16))) __bf16 bf16x16;
typedef __attribute__((ext_vector_type(4))) short s16x4;
typedef __amdgpu_buffer_rsrc_t rsrc_t;
typedef __attribute__((address_space(1))) u64 gu64;
#define AS3 __attribute__((address_space(3)))
#define DEV static __device__ __forceinline__

constexpr unsigned WS_KV = 0;
constexpr unsigned WS_O = 4u << 20;
constexpr unsigned WS_UT = 6u << 20;
constexpr unsigned WS_F = 8u << 20;
constexpr unsigned WS_EPOCH = WS_F;
constexpr unsigned WS_UFLAG = WS_F + 4096;
constexpr unsigned WS_KVFLAG = WS_UFLAG + 32768;
constexpr unsigned WS_USED = WS_KVFLAG + 32768;

constexpr unsigned XS = 0;
constexpr unsigned RING = 65536;
constexpr unsigned SS = 114688;
constexpr unsigned CH = 24496, O_UA = 16640, O_Y = 8192;
constexpr unsigned CNT = SS + 2 * CH;
constexpr unsigned C_X = CNT, C_KVD = CNT + 4, C_Q = CNT + 8, C_UD = CNT + 12, C_RDY = CNT + 16, C_URDY = CNT + 20, C_Y = CNT + 24, C_L = CNT + 32;

DEV rsrc_t mkrs(const void* p, unsigned bytes) { return __builtin_amdgcn_make_buffer_rsrc((void*)p, (short)0, (int)bytes, 0x00020000); }
DEV unsigned rfl(unsigned x) { return __builtin_amdgcn_readfirstlane(x); }
DEV unsigned pk2(float a, float b) { f32x2 v = {a, b}; return __builtin_bit_cast(unsigned, __builtin_convertvector(v, bf16x2)); }
DEV float lo16f(unsigned x) { return __builtin_bit_cast(float, x << 16); }
DEV float hi16f(unsigned x) { return __builtin_bit_cast(float, x & 0xffff0000u); }
DEV float rbf(float a) { return lo16f(pk2(a, a)); }
template <typename T> DEV T ldsr(unsigned a) { return *(const AS3 T*)a; }
template <typename T> DEV void ldsw(unsigned a, T v) { *(AS3 T*)a = v; }
DEV u32x2 ldtr(unsigned a) { return __builtin_bit_cast(u32x2, __builtin_amdgcn_ds_read_tr16_b64_v4i16((AS3 s16x4*)a)); }
DEV bf16x8 asb8(u32x4 v) { return __builtin_bit_cast(bf16x8, v); }
DEV u32x4 cat(u32x2 a, u32x2 b) { return u32x4{a[0], a[1], b[0], b[1]}; }
DEV f32x16 mfma32(u32x4 a, u32x4 b, f32x16 c) { return __builtin_amdgcn_mfma_f32_32x32x16_bf16(asb8(a), asb8(b), c, 0, 0, 0); }
DEV f32x4 mfma16(u32x4 a, u32x4 b, f32x4 c) { return __builtin_amdgcn_mfma_f32_16x16x32_bf16(asb8(a), asb8(b), c, 0, 0, 0); }

#define GLDS(lds, voff, srd, soff) \
  asm volatile("s_mov_b32 m0, %0\n\ts_nop 0\n\tbuffer_load_dwordx4 %1, %2, %3 offen lds" ::"s"(lds), "v"(voff), "s"(srd), "s"(soff) : "memory")
#define VMW(n) asm volatile("s_waitcnt vmcnt(%0)" ::"n"(n) : "memory")
#define LGW0() asm volatile("s_waitcnt lgkmcnt(0)" ::: "memory")
#define BAR() do { asm volatile("s_waitcnt lgkmcnt(0)\n\ts_barrier" ::: "memory"); } while (0)

#define SB() __builtin_amdgcn_sched_barrier(0)
#define OPQ(x) asm volatile("" : "+v"(x))
DEV void arrive(unsigned addr, int) {
  asm volatile("s_waitcnt lgkmcnt(0)\n\ts_mov_b64 exec, 1\n\tds_add_u32 %0, %1\n\ts_mov_b64 exec, -1" ::"v"(addr), "v"(1u) : "memory");
}
DEV void arrive_flag(unsigned addr, char* flag, u64 tag) {
  unsigned t, c;
  const u64 tv = tag;
  asm volatile("s_waitcnt lgkmcnt(0)\n\ts_mov_b64 exec, 1\n\tds_add_rtn_u32 %0, %2, %3\n\ts_waitcnt lgkmcnt(0)\n\t"
               "v_readfirstlane_b32 %1, %0\n\ts_nop 0\n\ts_cmp_eq_u32 %1, 3\n\ts_cbranch_scc0 1f\n\t"
               "global_store_dwordx2 %4, %5, %6 sc1\n1:\n\ts_mov_b64 exec, -1"
               : "=&v"(t), "=&s"(c) : "v"(addr), "v"(1u), "v"(0u), "v"(tv), "s"(flag) : "memory", "scc");
}
DEV void waitge(unsigned addr, unsigned target) {
  unsigned t, c;
  asm volatile("s_branch 2f\n1:\n\ts_sleep 1\n2:\n\tds_read_b32 %0, %2\n\ts_waitcnt lgkmcnt(0)\n\tv_readfirstlane_b32 %1, %0\n\ts_nop 0\n\t"
               "s_cmp_lt_u32 %1, %3\n\ts_cbranch_scc1 1b"
               : "=&v"(t), "=&s"(c) : "v"(addr), "n"(target) : "memory", "scc");
}
DEV void gsync(unsigned addr, unsigned target, int lane) { arrive(addr, lane); waitge(addr, target); }
DEV void pollflags(const char* base, unsigned off, u64 tag) {
  u64 t;
  unsigned n = 1u << 20;
  asm volatile("1:\n\tglobal_load_dwordx2 %0, %2, %3 sc1\n\ts_waitcnt vmcnt(0)\n\tv_cmp_ne_u64 vcc, %4, %0\n\ts_nop 4\n\t"
               "s_cmp_eq_u64 vcc, 0\n\ts_cbranch_scc1 2f\n\ts_sleep 2\n\ts_sub_u32 %1, %1, 1\n\ts_cbranch_scc0 1b\n2:"
               : "=&v"(t), "+s"(n) : "v"(off), "s"(base), "s"(tag) : "memory", "scc", "vcc");
}
DEV void setflag(char* p, u64 tag) { __hip_atomic_store((gu64*)p, tag, __ATOMIC_RELAXED, __HIP_MEMORY_SCOPE_AGENT); }
#define TAGWAIT() do { asm volatile("s_waitcnt lgkmcnt(0)" : "+s"(tag)::"memory"); tag = tag * 6364136223846793005ull + 1442695040888963407ull; } while (0)
DEV void st16sc1(u32x4 v, rsrc_t rs, unsigned voff, unsigned soff) {
  asm volatile("buffer_store_dwordx4 %0, %1, %2, %3 offen sc1\n\ts_nop 1" ::"v"(v), "v"(voff), "s"(rs), "s"(soff) : "memory");
}
DEV void st16(u32x4 v, rsrc_t rs, unsigned voff, unsigned soff) {
  asm volatile("buffer_store_dwordx4 %0, %1, %2, %3 offen\n\ts_nop 1" ::"v"(v), "v"(voff), "s"(rs), "s"(soff) : "memory");
}
DEV float xor32(float v) { return __shfl_xor(v, 32); }

typedef f32x2 cf;
DEV cf cmul(cf a, cf b) { return cf{__builtin_fmaf(-a[1], b[1], a[0] * b[0]), __builtin_fmaf(a[1], b[0], a[0] * b[1])}; }
DEV cf cexpl(float a2, float brev, float l) {
  float e = __builtin_amdgcn_exp2f(a2 * l);
  float ph = __builtin_amdgcn_fractf(brev * l);
  return cf{e * __builtin_amdgcn_cosf(ph), e * __builtin_amdgcn_sinf(ph)};
}

#define ST(k)

#define KVSC " sc1"
#define KVLOAD(b)                                                                                                   \
  do {                                                                                                              \
    asm volatile("buffer_load_dwordx4 %0, %3, %5, %6 offen" KVSC "\n\ts_mov_b64 exec, %7\n\t"                       \
                 "buffer_load_dwordx4 %1, %4, %5, %6 offen offset:1024" KVSC "\n\t"                                     \
                 "buffer_load_dwordx4 %2, %4, %5, %6 offen offset:1536" KVSC "\n\ts_mov_b64 exec, -1"                   \
                 : "+v"(kf[b]), "+v"(va[b]), "+v"(vb[b])                                                            \
                 : "v"(kvo), "v"(vvo), "s"(rws), "s"(so), "s"(lomask)                                               \
                 : "memory");                                                                                       \
    so += 2048; so = (so == soe) ? sob : so;                                                                        \
  } while (0)
#define QFLAG_AT 3
#define NOREFILL 0
#define MPRIO 2
#define KPRIO 3
#define PRIO_N0 6
#define KVWAIT(n, b) asm volatile("s_waitcnt vmcnt(%3)" : "+v"(kf[b]), "+v"(va[b]), "+v"(vb[b]) : "n"(n) : "memory")

DEV f32x16 smf(u32x4 a, f32x8 p, f32x16 acc) {
  return __builtin_amdgcn_smfmac_f32_32x32x32_bf16(asb8(a), __builtin_bit_cast(bf16x16, p), acc, (int)0xDDDDDDDD, 0, 0);
}
#define EX(d, j, s, k) d[j] = __builtin_amdgcn_exp2f(s[k])
#define TILE(S, SN, ACCP, VBP, PRE, KN, QN, POST, ACC, VA)                                            \
  {                                                                                                   \
    f32x8 p0;                                                                                         \
    EX(p0, 0, S, 0); EX(p0, 1, S, 1); SB();                                                           \
    ACCP = smf(VBP, ph, ACCP); SB();                                                                  \
    EX(p0, 2, S, 2); EX(p0, 3, S, 3); EX(p0, 4, S, 4); EX(p0, 5, S, 5); PRE; EX(p0, 6, S, 6); SB();   \
    SN = mfma32(KN, QN, zero16); SB(); POST; SB();                                                    \
    EX(p0, 7, S, 7); EX(ph, 0, S, 8); EX(ph, 1, S, 9); EX(ph, 2, S, 10); EX(ph, 3, S, 11); SB();      \
    ACC = smf(VA, p0, ACC); SB();                                                                     \
    EX(ph, 4, S, 12); EX(ph, 5, S, 13); EX(ph, 6, S, 14); EX(ph, 7, S, 15); SB();                     \
  }
DEV u32x4 pk8(const f32x16& a, int o) { return u32x4{pk2(a[o], a[o + 1]), pk2(a[o + 2], a[o + 3]), pk2(a[o + 4], a[o + 5]), pk2(a[o + 6], a[o + 7])}; }
DEV f32x4 sub4(const f32x16& a, int o) { return f32x4{a[o], a[o + 1], a[o + 2], a[o + 3]}; }

#if defined(__HIP_DEVICE_COMPILE__)
#define NOPK __attribute__((target("no-packed-fp32-ops")))
#else
#define NOPK
#endif
__global__ NOPK void __launch_bounds__(512, 2)
fused_kernel(const u16* __restrict__ pu, const u16* __restrict__ pAre, const u16* __restrict__ pAim, const u16* __restrict__ pBre,
             const u16* __restrict__ pBim, const u16* __restrict__ pCre, const u16* __restrict__ pCim, const u16* __restrict__ pLdt,
             const u16* __restrict__ pDsk, const u16* __restrict__ px, const u16* __restrict__ pW, const u16* __restrict__ pWo,
             u16* __restrict__ pout, char* __restrict__ ws) {
  __shared__ __attribute__((aligned(16))) char smem[163840];
  asm volatile("" ::"s"((unsigned)(size_t)(AS3 char*)smem));
#define ARGS() asm volatile("" ::"s"(pu), "s"(pAre), "s"(pAim), "s"(pBre), "s"(pBim), "s"(pCre), "s"(pCim), "s"(pLdt), "s"(pDsk), "s"(px), "s"(pW), "s"(pWo), "s"(pout), "s"(ws))
  ST(0);
  const int tid = threadIdx.x, lane = tid & 63;
  const unsigned w = rfl(tid >> 6);
  const unsigned hg = blockIdx.x & 7, rt = blockIdx.x >> 3;
  const unsigned ort = 4 * hg + (rt & 3), oct = rt >> 2;
  const unsigned r = lane & 31, hh = lane >> 5;
  ldsw<unsigned>(CNT + (tid & 15) * 4, 0u);
  ARGS();
  BAR();
  const rsrc_t rws = mkrs(ws, WS_USED);
  const rsrc_t rout = mkrs(pout, 2048 * 1024 * 2);
  u64 tag;
  u32x4 kf[2], va[2], vb[2], qf[4];
  const u64 lomask = 0x0000FFFF0000FFFFull;
  const unsigned kvo = lane * 16, vvo = hh * 256 + (lane & 15) * 16;
  const unsigned tp = rt >> 1, sv = rt & 1;
  const unsigned hs = w & 1, kq = w >> 1, head = hg * 4 + 2 * sv + hs;
  const unsigned sob = WS_KV + (head * 64 + kq * 16) * 2048, soe = sob + 16 * 2048;
  unsigned so = sob + tp * 2048;
  const f32x16 zero16 = {0};
#define KVINIT()                                                                                      \
  {                                                                                                   \
    const unsigned one = (lane & 27) == 16 ? 0x3f803f80u : 0u;    \
    _Pragma("unroll") for (int b = 0; b < 2; ++b) { kf[b] = u32x4{0, 0, 0, 0}; va[b] = u32x4{one, one, one, one}; vb[b] = va[b]; } \
  }
#define QLOAD() _Pragma("unroll") for (int t = 0; t < 4; ++t) qf[t] = ldsr<u32x4>(RING + 4096 + t * 12288 + hs * 1024 + kvo);

  if (w < 4) {
    const rsrc_t rx = mkrs(px, 2048 * 512 * 2), rw = mkrs(pW, 512 * 1536 * 2);
    const unsigned rr = lane >> 3, c8 = lane & 7;
    const unsigned xo0 = (128 * tp + 32 * w + rr) * 1024 + ((c8 ^ (rr >> 1)) << 4);
    const unsigned xo1 = (128 * tp + 32 * w + 8 + rr) * 1024 + ((c8 ^ (4 + (rr >> 1))) << 4);
    const unsigned wof = (16 * w + rr) * 3072 + 1024 + sv * 1024 + hg * 128 + ((c8 ^ (((rr >> 1) & 1) << 2)) << 4);
    const unsigned qof = (lane >> 2) * 3072 + hg * 128 + sv * 64 + (lane & 3) * 16;
    const unsigned ld0 = XS + w * 2048, rg = RING + w * 12288, rot = tp + 4 * sv;
#define SLAB(i) (((i) + rot) & 7)
#define LDW(i) do { GLDS(ld0 + (i) * 8192, wof, rw, SLAB(i) * 196608); GLDS(ld0 + (i) * 8192 + 1024, wof, rw, SLAB(i) * 196608 + 24576); } while (0)
#define LDX1(i, q)                                                                                    \
  if (NOREFILL && (i) >= 3) {} else if ((i) < 8) { GLDS(rg + ((i) % 3) * 4096 + (q) * 1024, ((q) & 1) ? xo1 : xo0, rx, SLAB(i) * 128 + ((q) >> 1) * 16384); } \
  else if ((i) < 10) { GLDS(rg + ((i) % 3) * 4096 + (q) * 1024, qof, rw, SLAB(2 * w + (i) - 8) * 196608 + (q) * 49152); }
#define LDX(i) do { LDX1(i, 0); LDX1(i, 1); LDX1(i, 2); LDX1(i, 3); } while (0)
    LDW(0); LDX(0); LDW(1); LDX(1); LDW(2); LDX(2); LDW(3); LDW(4); LDW(5); LDW(6); LDW(7);
    asm volatile("s_load_dwordx2 %0, %1, 0x0" : "=s"(tag) : "s"(ws + WS_EPOCH) : "memory");
    ST(1);
    unsigned xa[4];
#pragma unroll
    for (int ks = 0; ks < 4; ++ks) { xa[ks] = rg + r * 128 + (((2 * ks + hh) ^ ((r >> 1) & 7)) << 4); OPQ(xa[ks]); }
    const unsigned q4 = (lane & 15) >> 2;
    unsigned wa[2];
    wa[0] = XS + (8 * hh + q4) * 128 + ((((lane >> 4) & 1) * 32 + (lane & 3) * 8) ^ (((q4 >> 1) & 1) << 6));
    wa[1] = wa[0] ^ 64;
    OPQ(wa[0]); OPQ(wa[1]);
    f32x16 acc[2] = {zero16, zero16};
    u32x4 xf[8][4], fw[2][4];
#define GAP(i, ks, p, V)                                                                              \
  if ((i) > 0) { acc[p] = V ? mfma32(xf[(i) - 1][ks], fw[p][ks], acc[p]) : mfma32(fw[p][ks], xf[(i) - 1][ks], acc[p]); OPQ(acc[p]); } \
  SB();                                                                                               \
  if ((i) < 8) fw[p][ks] = cat(ldtr(wa[p] + (i) * 8192 + (ks) * 2048), ldtr(wa[p] + (i) * 8192 + (ks) * 2048 + 512)); \
  SB();
#define STEP(i, V)                                                                                    \
  if ((i) < 8) { _Pragma("unroll") for (int ks = 0; ks < 4; ++ks) xf[(i) & 7][ks] = ldsr<u32x4>(xa[ks] + ((i) % 3) * 4096); } \
  SB();                                                                                               \
  GAP(i, 0, 0, V); GAP(i, 0, 1, V); GAP(i, 1, 0, V); GAP(i, 1, 1, V);                                 \
  if ((i) < 7) asm volatile("s_waitcnt lgkmcnt(8)" ::: "memory");                                     \
  LDX1((i) + 3, 0); GAP(i, 2, 0, V); LDX1((i) + 3, 1); GAP(i, 2, 1, V); LDX1((i) + 3, 2); GAP(i, 3, 0, V); LDX1((i) + 3, 3); GAP(i, 3, 1, V);
#define VMW8 VMW(8)
#define KVPATH(V)                                                                                     \
  STEP(0, V);                                                                                         \
  VMW(20); STEP(1, V);                                                                                \
  VMW(16); gsync(C_X, 8, lane); STEP(2, V);                                                           \
  VMW8; arrive(C_X, lane); STEP(3, V);                                                              \
  ST(3);                                                                                              \
  VMW8; waitge(C_X, 12); STEP(4, V);                                                                \
  VMW8; STEP(5, V);                                                                                 \
  VMW8; STEP(6, V);                                                                                 \
  VMW8; STEP(7, V);                                                                                 \
  STEP(8, V);
    asm volatile("s_setprio %0" ::"n"(MPRIO));
    VMW(20); TAGWAIT(); gsync(C_X, 4, lane); ST(2);
    const unsigned kb = WS_KV + (hg * 4 * 64 + 4 * tp + w) * 2048;
    if (sv == 0) {
      KVPATH(0);
      const unsigned ko = (hh * 32 + ((r & 0x13) | ((r & 4) << 1) | ((r & 8) >> 1))) * 16;
      st16sc1(pk8(acc[0], 0), rws, ko, kb); st16sc1(pk8(acc[0], 8), rws, ko, kb + 131072);
      st16sc1(pk8(acc[1], 0), rws, ko, kb + 2 * 131072); st16sc1(pk8(acc[1], 8), rws, ko, kb + 3 * 131072);
    } else {
      KVPATH(1);
      const unsigned vo = (r >> 4) * 131072 + 1024 + vvo;
      st16sc1(pk8(acc[0], 0), rws, vo, kb); st16sc1(pk8(acc[0], 8), rws, vo, kb + 512);
      st16sc1(pk8(acc[1], 0), rws, vo, kb + 2 * 131072); st16sc1(pk8(acc[1], 8), rws, vo, kb + 2 * 131072 + 512);
    }
    ST(4);
    VMW(4); gsync(C_Q, 4, lane);
    {
      unsigned qa = RING + (8 * hh + q4) * 64 + ((lane >> 4) & 1) * 32 + (lane & 3) * 8;
      OPQ(qa);
      f32x16 aq = {0};
#define QRD(i, ks) fw[(i) & 1][ks] = cat(ldtr(qa + ((i) >> 1) * 12288 + (((i) & 1) ? 0 : 8192) + (ks) * 1024), ldtr(qa + ((i) >> 1) * 12288 + (((i) & 1) ? 0 : 8192) + (ks) * 1024 + 256))
      QRD(0, 0); QRD(0, 1); QRD(0, 2); QRD(0, 3);
      SB();
#pragma unroll
      for (int i = 0; i < 8; ++i) {
#pragma unroll
        for (int ks = 0; ks < 4; ++ks) {
          if (i < 7) QRD(i + 1, ks);
          SB();
          aq = mfma32(fw[i & 1][ks], xf[i][ks], aq); OPQ(aq);
          SB();
        }
        if (i == QFLAG_AT) {
          ST(5);
          VMW(0);
          arrive_flag(C_KVD, ws + WS_KVFLAG + (hg * 32 + rt) * 128, tag);
          ST(6);
        }
      }
      aq *= 0.25f * 1.44269504088896341f;
      ldsw<u32x4>(rg + 4096 + kvo, pk8(aq, 0)); ldsw<u32x4>(rg + 4096 + 1024 + kvo, pk8(aq, 8));
    }
    KVINIT();
    ST(19);
    gsync(C_Q, 8, lane);
    ST(20);
    QLOAD();
    ST(7);
    if (w == 0) { pollflags(ws + WS_KVFLAG, (hg * 32 + r) * 128, tag); arrive(C_RDY, lane); }
    else waitge(C_RDY, 1);
    ST(8);
    KVLOAD(0);
  } else {
    const unsigned v = w - 4, cc = v >> 1, a = v & 1;
    const unsigned d = 64 * hg + 2 * rt + cc;
    const unsigned cb0 = SS + cc * CH, tb = cb0 + a * 4352, ua = cb0 + O_UA, P = ua, cl = C_L + cc * 4;
    asm volatile("s_setprio 3");
    unsigned pa[6], pl, pd;
    {
      const unsigned po = (d * 128 + 64 * a + lane) * 2, pdo = d * 2;
      asm volatile("global_load_ushort %0, %1, %2" : "=v"(pl) : "v"(pdo), "s"(pLdt) : "memory");
      asm volatile("global_load_ushort %0, %1, %2" : "=v"(pa[0]) : "v"(po), "s"(pAre) : "memory");
      asm volatile("global_load_ushort %0, %1, %2" : "=v"(pa[1]) : "v"(po), "s"(pAim) : "memory");
      asm volatile("global_load_ushort %0, %1, %2" : "=v"(pa[2]) : "v"(po), "s"(pBre) : "memory");
      asm volatile("global_load_ushort %0, %1, %2" : "=v"(pa[3]) : "v"(po), "s"(pBim) : "memory");
      asm volatile("global_load_ushort %0, %1, %2" : "=v"(pa[4]) : "v"(po), "s"(pCre) : "memory");
      asm volatile("global_load_ushort %0, %1, %2" : "=v"(pa[5]) : "v"(po), "s"(pCim) : "memory");
      asm volatile("global_load_ushort %0, %1, %2" : "=v"(pd) : "v"(pdo), "s"(pDsk) : "memory");
    }
    {
      const rsrc_t ru = mkrs(pu, 2048 * 512 * 2);
      const unsigned uo = (64 * rt + (lane >> 1)) * 1024 + hg * 128 + v * 32 + (lane & 1) * 16;
      GLDS(tb, uo, ru, 0u); GLDS(tb + 1024, uo, ru, 32768u);
    }
    unsigned pf0 = 0;
    {
      u64 pc;
      unsigned len;
      asm volatile("s_getpc_b64 %0\n.Lpf_here:\n\ts_mov_b32 %1, .Lpf_end-.Lpf_here" : "=s"(pc), "=s"(len));
      const char* q;
      if (v == 0) q = (const char*)pc + min(((rt & 3) * 64 + lane) * 128, len);
      else q = (const char*)pW + (rt * 16 + (lane & 15)) * 3072 + hg * 128;
      asm volatile("global_load_dword %0, %1, off" : "=v"(pf0) : "v"(q) : "memory");
    }
    {
      const u32x4 ps = {~0u, ~0u, ~0u, ~0u};
      st16sc1(ps, rws, kvo, WS_O + ((4 * tp + v) * 32 + hg * 4 + 2 * sv) * 1024);
      st16sc1(ps, rws, kvo, WS_O + ((4 * tp + v) * 32 + hg * 4 + 2 * sv + 1) * 1024);
    }
    asm volatile("s_load_dwordx2 %0, %1, 0x0" : "=s"(tag) : "s"(ws + WS_EPOCH) : "memory");
    ST(1);
    asm volatile("s_waitcnt vmcnt(3)" : "+v"(pa[0]), "+v"(pa[1]), "+v"(pa[2]), "+v"(pa[3]), "+v"(pa[4]), "+v"(pa[5]), "+v"(pl), "+v"(pd)::"memory");
    TAGWAIT();
    ST(2);
    {
      const unsigned g = lane >> 4, ta = tb + (8 * g + ((lane & 15) >> 2)) * 32 + (lane & 3) * 8;
      u32x4 t0 = cat(ldtr(ta), ldtr(ta + 128)), t1 = cat(ldtr(ta + 1024), ldtr(ta + 1024 + 128));
      const unsigned go = (64 * hg + 16 * v + (lane & 15)) * 4096 + rt * 128 + g * 16;
      st16sc1(t0, rws, go, WS_UT);
      st16sc1(t1, rws, go + 64, WS_UT);
    }
    ST(23);
    asm volatile("s_setprio %0" ::"n"(KPRIO));
    const float dsk = lo16f(pd);
    {
      const float dt = rbf(expf(lo16f(pl)));
      const float a2 = dt * lo16f(pa[0]) * 1.44269504088896341f, br = dt * lo16f(pa[1]) * 0.15915494309189535f;
      const cf c = cmul(cf{lo16f(pa[4]), -lo16f(pa[5])}, cf{dt * lo16f(pa[2]), -dt * lo16f(pa[3])});
      const unsigned tw = tb + lane * 4;
      const unsigned tr = tb + (lane & 15) * 272 + (lane >> 4) * 16;
      const cf e1 = cexpl(a2, -br, 1.f), e256 = cexpl(a2, -br, 256.f), e8 = cexpl(a2, br, 8.f), e64 = cexpl(a2, br, 64.f);
      const cf e2 = cmul(e1, e1), e4 = cmul(e2, e2);
#define PUTG(row, g) do { cf g_ = (g); ldsw<unsigned>(tw + (row) * 272, pk2(g_[0], g_[1])); } while (0)
      u32x4 h0[4], h1[4];
      cf g256 = cmul(c, e256), g512 = cmul(g256, e256), g768 = cmul(g512, e256);
      {
        cf g1 = cmul(c, e1), g2 = cmul(c, e2), g4 = cmul(c, e4);
        cf g3 = cmul(g2, e1), g5 = cmul(g4, e1), g6 = cmul(g4, e2), g7 = cmul(g3, e4);
        PUTG(0, c); PUTG(1, g1); PUTG(2, g2); PUTG(3, g3); PUTG(4, g4); PUTG(5, g5); PUTG(6, g6); PUTG(7, g7);
        cf g258 = cmul(g256, e2), g260 = cmul(g256, e4), g262 = cmul(g258, e4);
        PUTG(8, g256); PUTG(9, g258); PUTG(10, g260); PUTG(11, g262);
        PUTG(12, g512); PUTG(13, cmul(g512, e4)); PUTG(14, g768); PUTG(15, cmul(g768, e4));
      }
#pragma unroll
      for (int ks = 0; ks < 4; ++ks) h0[ks] = ldsr<u32x4>(tr + ks * 64);
      asm volatile("" ::: "memory");
      {
        cf g = g768;
#pragma unroll
        for (int i = 0; i < 5; ++i) { g = cmul(g, e256); PUTG(i, g); }
      }
#pragma unroll
      for (int ks = 0; ks < 4; ++ks) h1[ks] = ldsr<u32x4>(tr + ks * 64);
      asm volatile("" ::: "memory");
      f32x4 dd[2][2];
      cf s0 = cf{1.f, 0.f}, s1 = e64;
      const cf e128 = cmul(e64, e64);
#pragma unroll
      for (int lt = 0; lt < 2; ++lt) {
        cf x0 = s0, x1 = s1;
#pragma unroll
        for (int i = 0; i < 8; ++i) {
          PUTG(i, x0); PUTG(8 + i, x1);
          if (i < 7) { x0 = cmul(x0, e8); x1 = cmul(x1, e8); }
        }
        u32x4 lf[4];
#pragma unroll
        for (int ks = 0; ks < 4; ++ks) lf[ks] = ldsr<u32x4>(tr + ks * 64);
        asm volatile(
            "v_mfma_f32_16x16x32_bf16 %0, %2, %6, 0\n\tv_mfma_f32_16x16x32_bf16 %1, %2, %10, 0\n\t"
            "v_mfma_f32_16x16x32_bf16 %0, %3, %7, %0\n\tv_mfma_f32_16x16x32_bf16 %1, %3, %11, %1\n\t"
            "v_mfma_f32_16x16x32_bf16 %0, %4, %8, %0\n\tv_mfma_f32_16x16x32_bf16 %1, %4, %12, %1\n\t"
            "v_mfma_f32_16x16x32_bf16 %0, %5, %9, %0\n\tv_mfma_f32_16x16x32_bf16 %1, %5, %13, %1\n\ts_nop 7"
            : "=&v"(dd[lt][0]), "=&v"(dd[lt][1])
            : "v"(lf[0]), "v"(lf[1]), "v"(lf[2]), "v"(lf[3]), "v"(h0[0]), "v"(h0[1]), "v"(h0[2]), "v"(h0[3]), "v"(h1[0]), "v"(h1[1]),
              "v"(h1[2]), "v"(h1[3])
            : "memory");
        s0 = e128; s1 = cmul(e128, e64);
        if (lt == 0) {
          ST(19);
          asm volatile("s_waitcnt vmcnt(0)" : "+v"(pf0)::"memory");
          ST(20);
          arrive_flag(C_UD, ws + WS_UFLAG + (hg * 32 + rt) * 128, tag);
          ST(21);
        }
      }
      const unsigned pw = P + a * 3024 + (lane & 15) * 144 + (lane >> 4) * 16;
      ST(22);
      ldsw<f32x4>(pw, dd[0][0]); ldsw<f32x4>(pw + 64, dd[1][0]);
      asm volatile("s_mov_b64 exec, %3\n\tds_write_b128 %0, %1 offset:2304\n\tds_write_b128 %0, %2 offset:2368\n\ts_mov_b64 exec, -1"
                   ::"v"(pw), "v"(dd[0][1]), "v"(dd[1][1]), "s"(0x001F001F001F001Full) : "memory");
    }
    ST(3);
    gsync(cl, 2, lane);
    {
      u32x4 dk[2];
      unsigned f0 = 0, f1 = 0;
      const unsigned lo = lane & 31;
#define RDP(ad) (ldsr<float>(ad) + ldsr<float>((ad) + 3024))
      const unsigned nx = lo < 31 ? 4 : 20;
      if (a == 0) {
        const unsigned b0 = P + lo * 4 + hh * 8 * 144, h1 = hh * 144;
        float t[8];
        t[0] = RDP(b0);
        t[1] = RDP(b0 + 144 - h1);
        t[2] = RDP(b0 + 2 * 144 - h1);
        t[3] = RDP(b0 + 3 * 144 - h1);
        t[4] = RDP(b0 + 4 * 144 - 2 * h1);
        t[5] = RDP(b0 + 5 * 144 - 3 * h1);
        t[6] = RDP(b0 + 6 * 144 - 3 * h1);
        t[7] = RDP(b0 + (hh ? (lo < 31 ? 4 : 20 + 3 * 144) : 7 * 144));
        dk[0] = u32x4{pk2(t[7], t[6]), pk2(t[5], t[4]), pk2(t[3], t[2]), pk2(t[1], t[0])};
        const unsigned b1 = P + (12 + 2 * hh) * 144 + lo * 4;
        float v0 = RDP(b1), v4 = RDP(b1 + 144), v8 = RDP(b1 + (lo < 31 ? 4 : 20 + 144));
        dk[1] = u32x4{pk2(v8, v8), pk2(v4, v4), pk2(v4, v0), pk2(v0, v0)};
      } else {
#pragma unroll
        for (int k = 0; k < 2; ++k) {
          const unsigned b1 = P + (16 + 2 * k + hh) * 144 + lo * 4;
          float v0 = RDP(b1), v8 = RDP(b1 + nx);
          dk[k] = u32x4{pk2(v8, v8), pk2(v8, (lane & 1) ? v8 : v0), pk2(v0, v0), pk2(v0, v0)};
        }
        float k1020 = RDP(P + 15 * 144 + 31 * 4);
        f0 = dk[0][3]; f1 = pk2(k1020, k1020);
      }
      arrive(cl, lane);
      const unsigned zo = cb0 + (255 - 128 * a - lane) * 16;
#pragma unroll
      for (int k = 0; k < 2; ++k) {
        const unsigned o = zo - 1024 * k;
        const unsigned n0 = __builtin_amdgcn_update_dpp(f0, dk[k][0], 0x138, 0xf, 0xf, false), n1 = __builtin_amdgcn_update_dpp(f1, dk[k][1], 0x138, 0xf, 0xf, false);
        unsigned e0 = __builtin_amdgcn_alignbit(dk[k][1], dk[k][0], 16), e1 = __builtin_amdgcn_alignbit(dk[k][2], dk[k][1], 16),
                 e2 = __builtin_amdgcn_alignbit(dk[k][3], dk[k][2], 16), e3 = __builtin_amdgcn_alignbit(n0, dk[k][3], 16),
                 e4 = __builtin_amdgcn_alignbit(n1, n0, 16);
        ldsw<u32x4>(o, dk[k]);
        ldsw<u32x4>(o + 4160, u32x4{e0, e1, e2, e3});
        ldsw<u32x4>(o + 8320, u32x4{dk[k][1], dk[k][2], dk[k][3], n0});
        ldsw<u32x4>(o + 12480, u32x4{e1, e2, e3, e4});
        f0 = __builtin_amdgcn_readlane(dk[0][0], 63); f1 = __builtin_amdgcn_readlane(dk[0][1], 63);
      }
      ldsw<u32x4>(cb0 + ((lane >> 2) & 3) * 4160 + 4096 + (lane & 3) * 16, u32x4{0, 0, 0, 0});
    }
    ST(4);
    if (v == 0) { pollflags(ws + WS_UFLAG, (lane < 32 ? hg * 32 + lane : (lane & 7) * 32 + (ort & ~1u) + ((lane >> 3) & 1)) * 128, tag); arrive(C_URDY, lane); }
    else waitge(C_URDY, 1);
    ST(5);
    {
      u32x4 u0, u1;
      const char* q = (const char*)pWo + ((rt * 4 + v) * 32 + r) * 128;
      asm volatile("buffer_load_dwordx4 %0, %3, %4, %5 offen sc1\n\tbuffer_load_dwordx4 %1, %3, %4, %5 offen offset:1024 sc1\n\t"
                   "global_load_dword %2, %6, off"
                   : "=&v"(u0), "=&v"(u1), "=&v"(pf0) : "v"(kvo), "s"(rws), "s"(WS_UT + d * 4096 + a * 2048), "v"(q) : "memory");
      waitge(cl, 4);
      ldsw<u32x4>(ua + (lane & 3) * 16, u32x4{0, 0, 0, 0});
      asm volatile("s_waitcnt vmcnt(1)" : "+v"(u0), "+v"(u1)::"memory");
      const unsigned uw = ua + (32 * a + (lane >> 2) + 1) * 80 + (lane & 3) * 16;
      ldsw<u32x4>(uw, u0); ldsw<u32x4>(uw + 16 * 80, u1);
    }
    gsync(cl, 6, lane);
    ST(6);
    f32x16 d0 = {0}, d1 = {0};
    {
      unsigned ab = cb0 + (3 - (r & 3)) * 4160 + 4088 + 16 * hh - 8 * (r >> 2) - 64 * (16 * a + 15) - 2048;
      const int ub = (int)(ua + 16 * hh), un = (int)(80 * (r + 1) - 80 * 16 * a);
      unsigned uh = ua + 16 * hh + 80 * (r + 33 - 16 * a - 15);
      OPQ(ab); OPQ(uh);
      u32x4 alo[2][2], ahi[2][2], bl[2][2], bh[2][2];
#define LDV2(ad) (*(const volatile AS3 u32x2*)(ad))
#define CVRD(mm)                                                                                      \
  {                                                                                                   \
    const int t = un - 80 * (mm);                                                                     \
    const unsigned ul = (unsigned)(ub + (t > 0 ? t : 0));                                             \
    _Pragma("unroll") for (int kk = 0; kk < 2; ++kk) {                                                \
      const unsigned o = ab + (15 - (mm)) * 64 + 32 * kk;                                             \
      alo[(mm) & 1][kk] = cat(LDV2(o + 2048), LDV2(o + 2048 + 8));                                    \
      bl[(mm) & 1][kk] = ldsr<u32x4>(ul + 32 * kk);                                                   \
      bh[(mm) & 1][kk] = ldsr<u32x4>(uh + (15 - (mm)) * 80 + 32 * kk);                                \
      ahi[(mm) & 1][kk] = cat(LDV2(o), LDV2(o + 8));                                                  \
    }                                                                                                 \
  }
      CVRD(0);
      asm volatile("s_setprio 0");
#pragma unroll
      for (int mm = 0; mm < 16; ++mm) {
        const int t = un - 80 * (mm + 1);
        const unsigned ul = (unsigned)(ub + (t > 0 ? t : 0));
        const int c = mm & 1, n = c ^ 1;
#pragma unroll
        for (int kk = 0; kk < 2; ++kk) {
          const unsigned o = ab + (14 - mm) * 64 + 32 * kk;
          SB(); d0 = mfma32(alo[c][kk], bl[c][kk], d0); SB();
          if (mm < 15) alo[n][kk] = cat(LDV2(o + 2048), LDV2(o + 2048 + 8));
          SB(); d1 = mfma32(alo[c][kk], bh[c][kk], d1); SB();
          if (mm < 15) { bl[n][kk] = ldsr<u32x4>(ul + 32 * kk); bh[n][kk] = ldsr<u32x4>(uh + (14 - mm) * 80 + 32 * kk); }
          SB(); d1 = mfma32(ahi[c][kk], bl[c][kk], d1); SB();
          if (mm < 15) ahi[n][kk] = cat(LDV2(o), LDV2(o + 8));
        }
      }
      asm volatile("s_setprio 3");
    }
    ST(7);
    KVINIT();
    asm volatile("s_waitcnt vmcnt(0)" : "+v"(pf0)::"memory");
    waitge(C_RDY, 1);
    KVLOAD(0);
    ST(8);
    {
      const f32x16 snd = a ? d0 : d1;
      f32x16 kp = a ? d1 : d0;
#pragma unroll
      for (int j = 0; j < 4; ++j) ldsw<f32x4>(RING + v * 12288 + 8192 + j * 1024 + lane * 16, sub4(snd, 4 * j));
      arrive(cl, lane);
      QLOAD();
      waitge(cl, 8);
      const unsigned yw = cb0 + O_Y + (32 * a + r) * 72 + hh * 8;
#pragma unroll
      for (int j = 0; j < 4; ++j) {
        f32x4 o = ldsr<f32x4>(RING + (v ^ 1) * 12288 + 8192 + j * 1024 + lane * 16);
        u32x2 uu = ldsr<u32x2>(ua + (32 * a + r + 1) * 80 + j * 16 + hh * 8);
        float uf[4] = {lo16f(uu[0]), hi16f(uu[0]), lo16f(uu[1]), hi16f(uu[1])}, y[4];
#pragma unroll
        for (int e = 0; e < 4; ++e) {
          unsigned ys = pk2(kp[4 * j + e] + o[e], uf[e] * dsk);
          y[e] = lo16f(ys) + hi16f(ys);
        }
        ldsw<u32x2>(yw + j * 16, u32x2{pk2(y[0], y[1]), pk2(y[2], y[3])});
      }
      gsync(C_Y, 4, lane);
    }
  }

  ST(9);
  f32x16 o[4] = {zero16, zero16, zero16, zero16};
  {
    const rsrc_t rwo = mkrs(pWo, 512 * 512 * 2);
    const unsigned oo = (64 * w + (lane >> 3)) * 1024 + oct * 128 + (((lane & 7) ^ (((lane >> 4) & 1) << 2)) << 4);
    const unsigned yo = lane * 2048 + (64 * hg + 2 * rt) * 2;
    unsigned ya = SS + O_Y + (16 * (w & 3) + hh) * 72 + r * 2, yv, yw;
    asm volatile("ds_read_u16 %0, %2\n\tds_read_u16 %1, %2 offset:%3" : "=&v"(yv), "=&v"(yw) : "v"(ya), "n"(CH) : "memory");
    KVWAIT(0, 0);
    f32x16 sa = mfma32(kf[0], qf[0], zero16), sb;
    f32x8 ph = {0, 0, 0, 0, 0, 0, 0, 0};
    ST(17);
    asm volatile("s_setprio 3");
    unsigned it = 0;
#pragma unroll 1
    for (unsigned seg = PRIO_N0; seg <= 8; ++seg) {
#pragma unroll 1
      do {
        GLDS(XS + w * 8192 + it * 1024, oo, rwo, it * 8192);
        if (w >= 4)
          asm volatile("s_waitcnt lgkmcnt(0)\n\tv_lshl_or_b32 %1, %1, 16, %0\n\tv_add_u32 %2, 0x90, %2\n\tbuffer_store_dword %1, %3, %4, %5 offen\n\t"
                       "ds_read_u16 %0, %2\n\tds_read_u16 %1, %2 offset:%6"
                       : "+v"(yv), "+v"(yw), "+v"(ya) : "v"(yo), "s"(rout), "s"((512 * (w & 3) + 64 * it) * 2048), "n"(CH) : "memory");
#pragma unroll
        for (int b = 0; b < 2; ++b) {
          TILE(sa, sb, o[3], vb[b ^ 1], , kf[b], qf[1], KVLOAD(b ^ 1), o[0], va[b]);
          TILE(sb, sa, o[0], vb[b], , kf[b], qf[2], , o[1], va[b]);
          TILE(sa, sb, o[1], vb[b], , kf[b], qf[3], , o[2], va[b]);
          TILE(sb, sa, o[2], vb[b], KVWAIT(0, b ^ 1), kf[b ^ 1], qf[0], , o[3], va[b]);
        }
        ++it;
      } while (it < seg);
      if (seg == PRIO_N0) asm volatile("s_setprio 2"); else asm volatile("s_setprio 0");
    }
    o[3] = smf(vb[1], ph, o[3]);
    asm volatile("s_waitcnt lgkmcnt(0)" : "+v"(yv), "+v"(yw)::"memory");
  }
  ST(10);
  {
    const unsigned oc = RING + hs * 27648;
    f32x8 ov8;
    float l;
    u32x4 q;
    waitge(C_Y, 4);
#pragma unroll
    for (int t = 0; t < 4; ++t) {
      if (t == kq) {
#pragma unroll
        for (int j = 0; j < 8; ++j) ov8[j] = o[t][j];
        l = o[t][8]; q = qf[t];
      } else {
        const unsigned ad = oc + t * 6912 + ((kq - t - 1) & 3) * 2304;
        ldsw<f32x4>(ad + kvo, sub4(o[t], 0)); ldsw<f32x4>(ad + 1024 + kvo, sub4(o[t], 4)); ldsw<float>(ad + 2048 + lane * 4, o[t][8]);
      }
    }
    BAR();
#pragma unroll
    for (int s = 0; s < 3; ++s) {
      const unsigned ad = oc + kq * 6912 + s * 2304;
      const f32x4 a0 = ldsr<f32x4>(ad + kvo), a1 = ldsr<f32x4>(ad + 1024 + kvo);
      l += ldsr<float>(ad + 2048 + lane * 4);
#pragma unroll
      for (int j = 0; j < 4; ++j) { ov8[j] += a0[j]; ov8[4 + j] += a1[j]; }
    }
    const float am = fmaxf(fmaxf(fmaxf(fabsf(ov8[0]), fabsf(ov8[1])), fmaxf(fabsf(ov8[2]), fabsf(ov8[3]))),
                           fmaxf(fmaxf(fabsf(ov8[4]), fabsf(ov8[5])), fmaxf(fabsf(ov8[6]), fabsf(ov8[7]))));
    if (__builtin_amdgcn_ballot_w64(!(l >= 0x1p-40f && l <= 0x1p40f && am <= 0x1p120f)) != 0) {
      const unsigned kb = WS_KV + head * 131072;
      float m = -INFINITY;
#pragma unroll 1
      for (int t = 0; t < 64; ++t) {
        f32x16 s = mfma32(__builtin_amdgcn_raw_buffer_load_b128(rws, kvo, kb + t * 2048, 16), q, zero16);
#pragma unroll
        for (int j = 0; j < 16; ++j) m = fmaxf(m, s[j]);
      }
      m = fmaxf(m, xor32(m));
      const unsigned one = (lane & 27) == 16 ? 0x3f803f80u : 0u;
      const u32x4 on4 = {one, one, one, one};
      f32x16 acc = {0};
#pragma unroll 1
      for (int t = 0; t < 64; ++t) {
        f32x16 s = mfma32(__builtin_amdgcn_raw_buffer_load_b128(rws, kvo, kb + t * 2048, 16), q, zero16);
        u32x4 x0 = __builtin_amdgcn_raw_buffer_load_b128(rws, vvo + 1024, kb + t * 2048, 16), x1 = __builtin_amdgcn_raw_buffer_load_b128(rws, vvo + 1536, kb + t * 2048, 16);
        if (lane & 16) { x0 = on4; x1 = on4; }
        f32x8 p0, p1;
#pragma unroll
        for (int j = 0; j < 8; ++j) { p0[j] = __builtin_amdgcn_exp2f(s[j] - m); p1[j] = __builtin_amdgcn_exp2f(s[8 + j] - m); }
        acc = smf(x0, p0, acc); acc = smf(x1, p1, acc);
      }
#pragma unroll
      for (int j = 0; j < 8; ++j) ov8[j] = acc[j];
      l = acc[8];
    }
    const float inv = __builtin_amdgcn_rcpf(l);
    u32x4 ov = {pk2(ov8[0] * inv, ov8[1] * inv), pk2(ov8[2] * inv, ov8[3] * inv), pk2(ov8[4] * inv, ov8[5] * inv), pk2(ov8[6] * inv, ov8[7] * inv)};
#pragma unroll
    for (int j = 0; j < 4; ++j) ov[j] = min(ov[j], 0xFFFFFFFEu);
    st16sc1(ov, rws, kvo, WS_O + ((4 * tp + kq) * 32 + head) * 1024);
  }
  {
    const unsigned qt = w & 1;
    const unsigned q4 = (lane & 15) >> 2, pq = lane & 3;
    const unsigned wa = XS + (128 * kq + 4 * hh + q4) * 128 + ((32 * ((lane >> 4) & 1) + 16 * (pq & 1) + 8 * (pq >> 1)) ^ (((q4 >> 1) & 1) << 6));
    ST(11);
    u32x4 af[2][8];
#pragma unroll
    for (int j = 0; j < 8; ++j) {
      af[0][j] = cat(ldtr(wa + j * 2048), ldtr(wa + j * 2048 + 1024));
      af[1][j] = cat(ldtr((wa ^ 64) + j * 2048), ldtr((wa ^ 64) + j * 2048 + 1024));
    }
    SB();
    u32x4 of[8];
    const unsigned ob = WS_O + ((2 * ort + qt) * 32 + 8 * kq) * 1024;
#pragma unroll
    for (int j = 0; j < 8; ++j) of[j] = __builtin_amdgcn_raw_buffer_load_b128(rws, kvo + j * 1024, ob, 16);
    SB();
    BAR();
    f32x16 acc0 = {0}, acc1 = {0};
#pragma unroll
    for (int g = 0; g < 2; ++g) {
      for (unsigned it = 0; it < (1u << 20); ++it) {
        unsigned m = 0;
#pragma unroll
        for (int j = 4 * g; j < 4 * g + 4; ++j) m = max(m, max(max(of[j][0], of[j][1]), max(of[j][2], of[j][3])));
        if (__builtin_amdgcn_ballot_w64(m == ~0u) == 0) break;
        asm volatile("s_sleep 2" ::: "memory");
#pragma unroll
        for (int j = 4 * g; j < 8; ++j) of[j] = __builtin_amdgcn_raw_buffer_load_b128(rws, kvo + j * 1024, ob, 16);
        asm volatile("" ::: "memory");
      }
#pragma unroll
      for (int j = 4 * g; j < 4 * g + 4; ++j) { acc0 = mfma32(af[0][j], of[j], acc0); acc1 = mfma32(af[1][j], of[j], acc1); }
    }
    ST(13);
    const unsigned rb = RING + qt * 24576 + lane * 16;
    f32x8 kp;
#pragma unroll
    for (int c = 0; c < 4; ++c) {
      const f32x4 x0 = sub4(c < 2 ? acc0 : acc1, 8 * (c & 1)), x1 = sub4(c < 2 ? acc0 : acc1, 8 * (c & 1) + 4);
      if (c == kq) {
        kp = f32x8{x0[0], x0[1], x0[2], x0[3], x1[0], x1[1], x1[2], x1[3]};
      } else {
        const unsigned ad = rb + c * 6144 + ((kq - c - 1) & 3) * 2048;
        ldsw<f32x4>(ad, x0); ldsw<f32x4>(ad + 1024, x1);
      }
    }
    BAR();
#pragma unroll
    for (int s = 0; s < 3; ++s) {
      const f32x4 a0 = ldsr<f32x4>(rb + kq * 6144 + s * 2048), a1 = ldsr<f32x4>(rb + kq * 6144 + s * 2048 + 1024);
#pragma unroll
      for (int j = 0; j < 4; ++j) { kp[j] += a0[j]; kp[4 + j] += a1[j]; }
    }
    u32x4 ov = {pk2(kp[0], kp[1]), pk2(kp[2], kp[3]), pk2(kp[4], kp[5]), pk2(kp[6], kp[7])};
    st16(ov, rout, r * 2048 + hh * 16, (64 * ort + 32 * qt) * 2048 + 1024 + (64 * oct + 16 * kq) * 2);
    asm volatile(".Lpf_end:");
    if (blockIdx.x == 0 && tid == 0) setflag(ws + WS_EPOCH, tag);
  }
}

extern "C" void kernel_launch(void* const* d_in, const int* in_sizes, int n_in, void* d_out, int out_size, void* d_ws, size_t ws_size,
                              hipStream_t stream) {
  fused_kernel<<<256, 512, 0, stream>>>((const u16*)d_in[0], (const u16*)d_in[1], (const u16*)d_in[2], (const u16*)d_in[3],
                                        (const u16*)d_in[4], (const u16*)d_in[5], (const u16*)d_in[6], (const u16*)d_in[7],
                                        (const u16*)d_in[8], (const u16*)d_in[9], (const u16*)d_in[10], (const u16*)d_in[11],
                                        (u16*)d_out, (char*)d_ws);
}
```

```cpp
#include <hip/hip_runtime.h>

typedef unsigned short u16;
typedef unsigned long long u64;
typedef __attribute__((ext_vector_type(2))) unsigned u32x2;
typedef __attribute__((ext_vector_type(4))) unsigned u32x4;
typedef __attribute__((ext_vector_type(2))) float f32x2;
typedef __attribute__((ext_vector_type(4))) float f32x4;
typedef __attribute__((ext_vector_type(8))) float f32x8;
typedef __attribute__((ext_vector_type(16))) float f32x16;
typedef __attribute__((ext_vector_type(2))) __bf16 bf16x2;
typedef __attribute__((ext_vector_type(8))) __bf16 bf16x8;
typedef __attribute__((ext_vector_type(16))) __bf16 bf16x16;
typedef __attribute__((ext_vector_type(4))) short s16x4;
typedef __amdgpu_buffer_rsrc_t rsrc_t;
typedef __attribute__((address_space(1))) u64 gu64;
#define AS3 __attribute__((address_space(3)))
#define DEV static __device__ __forceinline__

#define FS 128
constexpr unsigned WS_KV = 0;
constexpr unsigned WS_O = 4u << 20;
constexpr unsigned WS_UT = 6u << 20;
constexpr unsigned WS_F = 8u << 20;
constexpr unsigned WS_EPOCH = WS_F;
constexpr unsigned WS_UFLAG = WS_F + 4096;
constexpr unsigned WS_YFLAG = WS_UFLAG + 131072;
constexpr unsigned WS_KVFLAG = WS_YFLAG + 131072;
constexpr unsigned WS_USED = WS_KVFLAG + 256 * FS;

constexpr unsigned XS = 0;
constexpr unsigned RING = 65536;
constexpr unsigned SS = 114688;
constexpr unsigned CH = 24496, O_UA = 16640;
constexpr unsigned YL = RING + 73728;
constexpr unsigned CNT = SS + 2 * CH;
constexpr unsigned C_X = CNT, C_KVD = CNT + 4, C_Q = CNT + 8, C_RDY = CNT + 16, C_Y = CNT + 24, C_L = CNT + 32, C_W = CNT + 40, C_S = CNT + 44, C_U = CNT + 48;

DEV rsrc_t mkrs(const void* p, unsigned bytes) { return __builtin_amdgcn_make_buffer_rsrc((void*)p, (short)0, (int)bytes, 0x00020000); }
DEV unsigned rfl(unsigned x) { return __builtin_amdgcn_readfirstlane(x); }
DEV unsigned pk2(float a, float b) { f32x2 v = {a, b}; return __builtin_bit_cast(unsigned, __builtin_convertvector(v, bf16x2)); }
DEV float lo16f(unsigned x) { return __builtin_bit_cast(float, x << 16); }
DEV float hi16f(unsigned x) { return __builtin_bit_cast(float, x & 0xffff0000u); }
DEV float rbf(float a) { return lo16f(pk2(a, a)); }
template <typename T> DEV T ldsr(unsigned a) { return *(const AS3 T*)a; }
template <typename T> DEV void ldsw(unsigned a, T v) { *(AS3 T*)a = v; }
DEV u32x2 ldtr(unsigned a) { return __builtin_bit_cast(u32x2, __builtin_amdgcn_ds_read_tr16_b64_v4i16((AS3 s16x4*)a)); }
DEV bf16x8 asb8(u32x4 v) { return __builtin_bit_cast(bf16x8, v); }
DEV u32x4 cat(u32x2 a, u32x2 b) { return u32x4{a[0], a[1], b[0], b[1]}; }
DEV f32x16 mfma32(u32x4 a, u32x4 b, f32x16 c) { return __builtin_amdgcn_mfma_f32_32x32x16_bf16(asb8(a), asb8(b), c, 0, 0, 0); }
DEV f32x4 mfma16(u32x4 a, u32x4 b, f32x4 c) { return __builtin_amdgcn_mfma_f32_16x16x32_bf16(asb8(a), asb8(b), c, 0, 0, 0); }

#define NT1 ""
#define NT2 ""
#define GLDSX(lds, voff, srd, soff) \
  asm volatile("s_mov_b32 m0, %0\n\ts_nop 0\n\tbuffer_load_dwordx4 %1, %2, %3 offen " NT2 " lds" ::"s"(lds), "v"(voff), "s"(srd), "s"(soff) : "memory")
#define GLDS(lds, voff, srd, soff) \
  asm volatile("s_mov_b32 m0, %0\n\ts_nop 0\n\tbuffer_load_dwordx4 %1, %2, %3 offen " NT1 " lds" ::"s"(lds), "v"(voff), "s"(srd), "s"(soff) : "memory")
#define SETM0(base, imm) asm volatile("s_add_u32 m0, %0, %1" ::"s"(base), "n"(imm) : "memory", "scc")
#define GLDS2(voff, srd, soff)  asm volatile("buffer_load_dwordx4 %0, %1, %2 offen " NT2 " lds" ::"v"(voff), "s"(srd), "s"(soff) : "memory")
#define VMW(n) asm volatile("s_waitcnt vmcnt(%0)" ::"n"(n) : "memory")
#define LGW0() asm volatile("s_waitcnt lgkmcnt(0)" ::: "memory")
#define BAR() do { asm volatile("s_waitcnt lgkmcnt(0)\n\ts_barrier" ::: "memory"); } while (0)

#define SB() __builtin_amdgcn_sched_barrier(0)
#define OPQ(x) asm volatile("" : "+v"(x))
DEV void arrive(unsigned addr, int) {
  asm volatile("s_waitcnt lgkmcnt(0)\n\ts_mov_b64 exec, 1\n\tds_add_u32 %0, %1\n\ts_mov_b64 exec, -1" ::"v"(addr), "v"(1u) : "memory");
}
template <int LAST> DEV void arrive_flag(unsigned addr, char* flag, u64 tag) {
  unsigned t, c;
  const u64 tv = tag;
  asm volatile("s_mov_b64 exec, 1\n\tds_add_rtn_u32 %0, %2, %3\n\ts_waitcnt lgkmcnt(0)\n\t"
               "v_readfirstlane_b32 %1, %0\n\ts_nop 0\n\ts_cmp_eq_u32 %1, %7\n\ts_cbranch_scc0 1f\n\t"
               "global_store_dwordx2 %4, %5, %6 sc1\n1:\n\ts_mov_b64 exec, -1"
               : "=&v"(t), "=&s"(c) : "v"(addr), "v"(1u), "v"(0u), "v"(tv), "s"(flag), "n"(LAST) : "memory", "scc");
}
DEV void putflag(char* flag, u64 tag) {
  const u64 tv = tag;
  asm volatile("s_mov_b64 exec, 1\n\tglobal_store_dwordx2 %0, %1, %2 sc1\n\ts_mov_b64 exec, -1" ::"v"(0u), "v"(tv), "s"(flag) : "memory");
}
#define FLAGLD(d, off, base) asm volatile("global_load_dwordx2 %0, %1, %2 sc1" : "=v"(d) : "v"(off), "s"(base) : "memory")
#define FLAGCK(d, off, base) do { asm volatile("" : "+v"(d)); if (__builtin_amdgcn_ballot_w64(d != tag) != 0) pollflags(base, off, tag); } while (0)
DEV void waitge(unsigned addr, unsigned target) {
  unsigned t, c;
  asm volatile("s_branch 2f\n1:\n\ts_sleep 1\n2:\n\tds_read_b32 %0, %2\n\ts_waitcnt lgkmcnt(0)\n\tv_readfirstlane_b32 %1, %0\n\ts_nop 0\n\t"
               "s_cmp_lt_u32 %1, %3\n\ts_cbranch_scc1 1b"
               : "=&v"(t), "=&s"(c) : "v"(addr), "n"(target) : "memory", "scc");
}
DEV void waitge_pre(unsigned pre, unsigned addr, unsigned target) {
  unsigned c;
  asm volatile("v_readfirstlane_b32 %1, %0\n\ts_nop 0\n\ts_cmp_ge_u32 %1, %3\n\ts_cbranch_scc1 2f\n"
               "1:\n\tds_read_b32 %0, %2\n\ts_waitcnt lgkmcnt(0)\n\tv_readfirstlane_b32 %1, %0\n\ts_nop 0\n\t"
               "s_cmp_lt_u32 %1, %3\n\ts_cbranch_scc1 1b\n2:"
               : "+v"(pre), "=&s"(c) : "v"(addr), "n"(target) : "memory", "scc");
}
DEV void arrive_nw(unsigned addr) {
  asm volatile("s_mov_b64 exec, 1\n\tds_add_u32 %0, %1\n\ts_mov_b64 exec, -1" ::"v"(addr), "v"(1u) : "memory");
}
DEV void gsync(unsigned addr, unsigned target, int lane) { arrive(addr, lane); waitge(addr, target); }
DEV void pollflags(const char* base, unsigned off, u64 tag) {
  u64 t;
  unsigned n = 1u << 20;
  asm volatile("1:\n\tglobal_load_dwordx2 %0, %2, %3 sc1\n\ts_waitcnt vmcnt(0)\n\tv_cmp_ne_u64 vcc, %4, %0\n\ts_nop 4\n\t"
               "s_cmp_eq_u64 vcc, 0\n\ts_cbranch_scc1 2f\n\ts_sleep 2\n\ts_sub_u32 %1, %1, 1\n\ts_cbranch_scc0 1b\n2:"
               : "=&v"(t), "+s"(n) : "v"(off), "s"(base), "s"(tag) : "memory", "scc", "vcc");
}
template <int N, int STRIDE> DEV void spull(const char* p) {
#pragma unroll
  for (int i = 0; i < N; ++i) asm volatile("s_load_dword s100, %0, %1" ::"s"(p), "n"(i * STRIDE) : "memory", "s100");
}
DEV void setflag(char* p, u64 tag) { __hip_atomic_store((gu64*)p, tag, __ATOMIC_RELAXED, __HIP_MEMORY_SCOPE_AGENT); }
#define TAGLOAD() asm volatile("s_load_dwordx2 s[94:95], %0, 0x0" ::"s"(ws + WS_EPOCH) : "memory", "s94", "s95")
#define TAGWAIT() do { asm volatile("s_waitcnt lgkmcnt(0)\n\ts_mov_b64 %0, s[94:95]" : "=s"(tag)::"memory"); tag = tag * 6364136223846793005ull + 1442695040888963407ull; } while (0)
DEV void st16sc1(u32x4 v, rsrc_t rs, unsigned voff, unsigned soff) {
  asm volatile("buffer_store_dwordx4 %0, %1, %2, %3 offen sc1\n\ts_nop 1" ::"v"(v), "v"(voff), "s"(rs), "s"(soff) : "memory");
}
#define OUTMOD " sc1"
DEV void st16(u32x4 v, rsrc_t rs, unsigned voff, unsigned soff) {
  asm volatile("buffer_store_dwordx4 %0, %1, %2, %3 offen" OUTMOD "\n\ts_nop 1" ::"v"(v), "v"(voff), "s"(rs), "s"(soff) : "memory");
}
DEV float xor32(float v) { return __shfl_xor(v, 32); }

typedef f32x2 cf;
DEV cf cmul(cf a, cf b) { return cf{__builtin_fmaf(-a[1], b[1], a[0] * b[0]), __builtin_fmaf(a[1], b[0], a[0] * b[1])}; }
DEV cf cexpl(float a2, float brev, float l) {
  float e = __builtin_amdgcn_exp2f(a2 * l);
  float ph = __builtin_amdgcn_fractf(brev * l);
  return cf{e * __builtin_amdgcn_cosf(ph), e * __builtin_amdgcn_sinf(ph)};
}

#define ST(k)
#define STIT()
#define STFL()

#define KVSC " sc1"
#define KVLOAD(b)                                                                                                   \
  do {                                                                                                              \
    asm volatile("buffer_load_dwordx4 %0, %3, %5, %6 offen" KVSC "\n\ts_mov_b64 exec, %7\n\t"                       \
                 "buffer_load_dwordx4 %1, %4, %5, %6 offen offset:1024" KVSC "\n\t"                                     \
                 "buffer_load_dwordx4 %2, %4, %5, %6 offen offset:1536" KVSC "\n\ts_mov_b64 exec, -1"                   \
                 : "+v"(kf[b]), "+v"(va[b]), "+v"(vb[b])                                                            \
                 : "v"(kvo), "v"(vvo), "s"(rws), "s"(so), "s"(lomask)                                               \
                 : "memory");                                                                                       \
    if (!XC) { so += 2048; so = (so == soe) ? sob : so; }                                                                        \
  } while (0)
#define XC 0
#define HP2 9
#define OSL 2
#define QD 1
#define QFLAG_AT 20
#define NOREFILL 0
#define MPRIO 2
#define MAPT 1
#define CPRIO 0
#define XST 0
#define PL_0 3
#define PL_C 3
#define PL_LAST 1
#define UST 1
#define PL_AFTER 1
#define UPF 0
#define J4 8
#define J3 3
#define WOPF 0
#define WPRIO 3
#define KSIG 7
#define EPRIO 3
#define WPOS 0
#define WFP 6
#define UPOS 21
#define UTP 9
#define UFP 15
#define ROT4 0
#define PFX 0
#define LPRIO0 3
#define KPRIO 0
#define PM_SEG 0x180
#define PL_SEG 0x182
#define PM_LVL 0x02
#define PL_LVL 0x133
#define KVWAIT(n, b) asm volatile("s_waitcnt vmcnt(%3)" : "+v"(kf[b]), "+v"(va[b]), "+v"(vb[b]) : "n"(n) : "memory")

DEV f32x16 smf(u32x4 a, f32x8 p, f32x16 acc) {
  return __builtin_amdgcn_smfmac_f32_32x32x32_bf16(asb8(a), __builtin_bit_cast(bf16x16, p), acc, (int)0xDDDDDDDD, 0, 0);
}
#define EX(d, j, s, k) d[j] = __builtin_amdgcn_exp2f(s[k])
#define TILE(S, SN, ACCP, VBP, PRE, KN, QN, POST, ACC, VA, CV, g)                                     \
  {                                                                                                   \
    f32x8 p0;                                                                                         \
    CV(0, g); EX(p0, 0, S, 0); EX(p0, 1, S, 1); SB();                                                 \
    ACCP = smf(VBP, ph, ACCP); SB();                                                                  \
    EX(p0, 2, S, 2); EX(p0, 3, S, 3); EX(p0, 4, S, 4); CV(1, g); EX(p0, 5, S, 5); PRE; EX(p0, 6, S, 6); SB(); \
    SN = mfma32(KN, QN, zero16); SB(); POST; SB();                                                    \
    EX(p0, 7, S, 7); EX(ph, 0, S, 8); if (HP2 == 9) { CV(2, g); } EX(ph, 1, S, 9); if (HP2 == 10) { CV(2, g); } EX(ph, 2, S, 10); EX(ph, 3, S, 11); SB(); \
    ACC = smf(VA, p0, ACC); SB();                                                                     \
    EX(ph, 4, S, 12); EX(ph, 5, S, 13); CV(3, g); EX(ph, 6, S, 14); EX(ph, 7, S, 15); SB();           \
  }
#define HALF(b, CV, H, N)                                                                             \
  TILE(sa, sb, o[3], vb[(b) ^ 1], , kf[b], qf[1], KVLOAD((b) ^ 1); H, o[0], va[b], CV, 4 * (b));      \
  TILE(sb, sa, o[0], vb[b], , kf[b], qf[2], , o[1], va[b], CV, 4 * (b) + 1);                          \
  TILE(sa, sb, o[1], vb[b], , kf[b], qf[3], , o[2], va[b], CV, 4 * (b) + 2);                          \
  TILE(sb, sa, o[2], vb[b], KVWAIT(N, (b) ^ 1), kf[(b) ^ 1], qf[0], , o[3], va[b], CV, 4 * (b) + 3);
#define FPOS(p, C, H) if ((p) == (C) - 1) FLAGLD(ff, ffo, ws + WS_KVFLAG); if ((p) == (C)) { VMW(0); FLAGCK(ff, ffo, ws + WS_KVFLAG); ST(21); KVLOAD(1); H; }
#define HALF0(C, H, N)                                                                                \
  TILE(sa, sb, o[3], vb[1], , kf[0], qf[1], FPOS(0, C, H), o[0], va[0], NOCV, 0);                     \
  TILE(sb, sa, o[0], vb[0], , kf[0], qf[2], FPOS(1, C, H), o[1], va[0], NOCV, 0);                     \
  TILE(sa, sb, o[1], vb[0], , kf[0], qf[3], FPOS(2, C, H), o[2], va[0], NOCV, 0);                     \
  TILE(sb, sa, o[2], vb[0], FPOS(3, C, H); KVWAIT(N, 1), kf[1], qf[0], , o[3], va[0], NOCV, 0);
#define ITER(CV, H0, N0, H1, N1) STIT(); HALF(0, CV, WOLD(); H0, (N0) + 1); HALF(1, CV, H1, N1); ++it;
#define NOCV(n, g)
DEV u32x4 pk8(const f32x16& a, int o) { return u32x4{pk2(a[o], a[o + 1]), pk2(a[o + 2], a[o + 3]), pk2(a[o + 4], a[o + 5]), pk2(a[o + 6], a[o + 7])}; }
DEV f32x4 sub4(const f32x16& a, int o) { return f32x4{a[o], a[o + 1], a[o + 2], a[o + 3]}; }

#if defined(__HIP_DEVICE_COMPILE__)
#define NOPK __attribute__((target("no-packed-fp32-ops")))
#else
#define NOPK
#endif
__global__ NOPK void __launch_bounds__(512, 2)
fused_kernel(const u16* __restrict__ pu, const u16* __restrict__ pAre, const u16* __restrict__ pAim, const u16* __restrict__ pBre,
             const u16* __restrict__ pBim, const u16* __restrict__ pCre, const u16* __restrict__ pCim, const u16* __restrict__ pLdt,
             const u16* __restrict__ pDsk, const u16* __restrict__ px, const u16* __restrict__ pW, const u16* __restrict__ pWo,
             u16* __restrict__ pout, char* __restrict__ ws) {
  __shared__ __attribute__((aligned(16))) char smem[163840];
  asm volatile("" ::"s"((unsigned)(size_t)(AS3 char*)smem));
#define ARGS() asm volatile("" ::"s"(pu), "s"(pAre), "s"(pAim), "s"(pBre), "s"(pBim), "s"(pCre), "s"(pCim), "s"(pLdt), "s"(pDsk), "s"(px), "s"(pW), "s"(pWo), "s"(pout), "s"(ws))
  ST(0);
  const int tid = threadIdx.x, lane = tid & 63;
  const unsigned w = rfl(tid >> 6);
  const unsigned xc = blockIdx.x & 7, xi = blockIdx.x >> 3;
  const unsigned hg = xc, rt = xi;
  const unsigned ort = 4 * xc + (xi & 3), oct = xi >> 2;
  const unsigned r = lane & 31, hh = lane >> 5;
  ldsw<unsigned>(CNT + (tid & 15) * 4, 0u);
  ARGS();
  BAR();
  const rsrc_t rws = mkrs(ws, WS_USED);
  const rsrc_t rout = mkrs(pout, 2048 * 1024 * 2);
  u64 tag;
  u32x4 kf[2], va[2], vb[2], qf[4];
  const u64 lomask = 0x0000FFFF0000FFFFull;
  const unsigned kvo = lane * 16, vvo = hh * 256 + (lane & 15) * 16;
  const unsigned tp = rt >> 1, sv = rt & 1;
  const unsigned hs = w & 1, kq = w >> 1, head = hg * 4 + 2 * sv + hs;
  const unsigned sob = WS_KV + head * 131072, soe = sob + 131072, t0 = (4 * tp + 4 + 15 * kq) & 63;
#define XA 0
#define XB 0
#define PSTEP 7
#define PL_6 PL_AFTER
#define UFH 0
#define FCM 1
#define FCL 0
#define FLT 6
#define PM_0 2
#define Q2 0
#define PL_1 3
#define PL_4 3
#define PL_5 3
#define KWARM 1
#define KPF 0
#define KROT 12
  unsigned so = sob + t0 * 2048;
  const f32x16 zero16 = {0};
#define KVINIT()                                                                                      \
  {                                                                                                   \
    const unsigned one = (lane & 27) == 16 ? 0x3f803f80u : 0u;    \
    _Pragma("unroll") for (int b = 0; b < 2; ++b) { kf[b] = u32x4{0, 0, 0, 0}; va[b] = u32x4{one, one, one, one}; vb[b] = va[b]; } \
  }
#define QLOAD() _Pragma("unroll") for (int t = 0; t < 4; ++t) qf[t] = ldsr<u32x4>(RING + 4096 + t * 12288 + hs * 1024 + kvo);

  const rsrc_t rw = mkrs(pW, 512 * 1536 * 2);
  const unsigned rr = lane >> 3, c8 = lane & 7, rot = tp + ROT4 * sv;
  const unsigned wof = (16 * (w & 3) + rr) * 3072 + 1024 + (((c8 >> 2) ^ (rr >> 1)) & 1) * 1024 + hg * 128 + sv * 64 + (c8 & 3) * 16, ld0 = XS + (w & 3) * 2048;
#define SLAB(i) (((i) + rot) & 7)
#define LDW(i) do { GLDS(ld0 + (i) * 8192, wof, rw, SLAB(i) * 196608); GLDS(ld0 + (i) * 8192 + 1024, wof, rw, SLAB(i) * 196608 + 24576); } while (0)
  f32x16 o[4];
#define ATT_BEGIN()                                                                                   \
  ST(9);                                                                                              \
  o[0] = o[1] = o[2] = o[3] = zero16;                                                                 \
  const rsrc_t rwo = mkrs(pWo, 512 * 512 * 2);                                                        \
  const unsigned oo = (64 * w + (lane >> 3)) * 1024 + oct * 128 + (((lane & 7) ^ (((lane >> 4) & 1) << 2)) << 4); \
  asm volatile("s_waitcnt lgkmcnt(0)" : "+v"(kf[0]), "+v"(va[0]), "+v"(vb[0])::"memory");                                                                               \
  f32x16 sa = mfma32(kf[0], qf[0], zero16), sb;                                                       \
  f32x8 ph = {0, 0, 0, 0, 0, 0, 0, 0};                                                                \
  unsigned it = 0;                                                                                    \
  ST(17);
#define POISON(n) st16sc1(u32x4{~0u, ~0u, ~0u, ~0u}, rws, kvo, WS_O + ((4 * tp + kq + (n)) * 32 + head) * 1024)
#define KVFIRST(C)                                                                                    \
  u64 ff;                                                                                             \
  const unsigned ffo = (hg * 32 + (((t0 + (lane & 1)) & 63) >> 2) * 2 + sv) * FS;                     \
  FPOS(-1, C, );                                                                                      \
  waitge(C_Q, 8);                                                                                     \
  asm volatile("ds_read_b128 %0, %3\n\ts_mov_b64 exec, %5\n\tds_read_b128 %1, %4 offset:1024\n\tds_read_b128 %2, %4 offset:1536\n\ts_mov_b64 exec, -1" \
               : "+v"(kf[0]), "+v"(va[0]), "+v"(vb[0]) : "v"(XS + w * 8192 + 4096 + kvo), "v"(XS + w * 8192 + 4096 + vvo), "s"(lomask) : "memory"); \
  QLOAD();
#define WOLD() GLDS(XS + w * 8192 + it * 1024, XB ? kvo : oo, XB ? rws : rwo, XB ? so : it * 8192)
#define PRIO_AT(i, p) asm volatile("s_cmp_lg_u32 %0, %1\n\ts_cbranch_scc1 1f\n\ts_setprio %2\n1:" ::"s"(it), "n"(i), "n"(p) : "scc")
  if (w < 4) {
    const rsrc_t rx = mkrs(px, 2048 * 512 * 2);
    unsigned xo[4], qo[4];
#pragma unroll
    for (int q = 0; q < 4; ++q) {
      xo[q] = (128 * tp + 32 * w + 8 * q + rr) * 1024 + ((c8 ^ (4 * (q & 1) + (rr >> 1))) << 4);
      qo[q] = (16 * q + (lane >> 2)) * 3072 + hg * 128 + sv * 64 + (lane & 3) * 16;
    }
    const unsigned rg = RING + w * 12288;
#define LDXM(i, q) if ((i) < 10) SETM0(rg, ((i) % 3) * 4096 + (q) * 1024)
#define LDX1(i, q)                                                                                    \
  if (NOREFILL && (i) >= 3) {} else if ((i) < 8) { GLDS2(xo[q], rx, SLAB(i) * 128); }                 \
  else if ((i) < 10) { GLDS2(qo[q], rw, SLAB(2 * w + (i) - 8) * 196608); }
#define LDX(i) _Pragma("unroll") for (int q = 0; q < 4; ++q) GLDSX(rg + (i) * 4096 + q * 1024, xo[q], rx, SLAB(i) * 128)
    asm volatile("s_setprio %0" ::"n"(MPRIO));
    LDW(0); LDX(0); LDW(1); LDX(1); LDW(2); LDX(2);
    unsigned cnt = 0;
    TAGLOAD();
    ST(1);
    unsigned xa[4];
#pragma unroll
    for (int ks = 0; ks < 4; ++ks) { xa[ks] = rg + r * 128 + (((2 * ks + hh) ^ ((r >> 1) & 7)) << 4); OPQ(xa[ks]); }
    const unsigned q4 = (lane & 15) >> 2;
    unsigned wa[2];
    wa[0] = XS + (8 * hh + q4) * 128 + ((((lane >> 4) & 1) * 32 + (lane & 3) * 8) ^ (((q4 >> 1) & 1) << 6));
    wa[1] = wa[0] ^ 64;
    OPQ(wa[0]); OPQ(wa[1]);
    f32x16 acc[2] = {zero16, zero16};
    u32x4 xf[8][4], fw[2][4];
#define GAP(i, ks, p, V, MID, END)                                                                    \
  if ((i) > 0) { acc[p] = p ? mfma32(xf[(i) - 1][ks], fw[p][ks], acc[p]) : mfma32(fw[p][ks], xf[(i) - 1][ks], acc[p]); OPQ(acc[p]); } \
  SB(); MID; SB();                                                                                    \
  if ((i) < 8) fw[p][ks] = cat(ldtr(wa[p] + (i) * 8192 + (ks) * 2048), ldtr(wa[p] + (i) * 8192 + (ks) * 2048 + 512)); \
  SB(); END; SB();
#define XRD(i, ks) if ((i) < 8) xf[(i) & 7][ks] = ldsr<u32x4>(xa[ks] + ((i) % 3) * 4096)
#define STEP(i, V)                                                                                    \
  XRD(i, 0); XRD(i, 1);                                                                               \
  SB();                                                                                               \
  GAP(i, 0, 0, V, XRD(i, 2); XRD(i, 3); SB(); if ((i) == 2) waitge_pre(cnt, C_X, 8); if ((i) == 3) waitge_pre(cnt, C_W, 4); if ((i) == 1) arrive_nw(C_X); if ((i) == KSIG) arrive_nw(C_S), ); \
  GAP(i, 0, 1, V, , );                                                                                \
  GAP(i, 1, 0, V, , );                                                                                \
  GAP(i, 1, 1, V, if ((i) < 7) asm volatile("s_waitcnt lgkmcnt(6)" ::: "memory"); LDXM((i) + 3, 0), LDX1((i) + 3, 0)); \
  GAP(i, 2, 0, V, LDXM((i) + 3, 1), LDX1((i) + 3, 1));                                                \
  GAP(i, 2, 1, V, if ((i) == 1) cnt = *(const volatile AS3 unsigned*)C_X; if ((i) == 2) cnt = *(const volatile AS3 unsigned*)C_W, ); \
  GAP(i, 3, 0, V, LDXM((i) + 3, 2), LDX1((i) + 3, 2));                                                \
  GAP(i, 3, 1, V, LDXM((i) + 3, 3), LDX1((i) + 3, 3));
#define KVPATH(V)                                                                                     \
  STEP(0, V);                                                                                         \
  ST(24); VMW(8); ST(25); STEP(1, V);                                                                 \
  ST(26); VMW(8); ST(27); STEP(2, V);                                                                 \
  ST(28); VMW(8); ST(29); STEP(3, V);                                                                 \
  ST(30); VMW(8); ST(31); STEP(4, V);                                                                 \
  ST(32); VMW(8); ST(33); STEP(5, V);                                                                 \
  ST(34); VMW(8); ST(35); STEP(6, V);                                                                 \
  ST(36); VMW(8); ST(37); STEP(7, V);                                                                 \
  ST(38); STEP(8, V); ST(39); STFL();
    VMW(10); TAGWAIT(); gsync(C_X, 4, lane); ST(2);
    const unsigned kb = WS_KV + ((hg * 4 + 2 * sv) * 64 + 4 * tp + w) * 2048;
    KVPATH(0);
    {
      const unsigned ko = (hh * 32 + ((r & 0x13) | ((r & 4) << 1) | ((r & 8) >> 1))) * 16, vo = (r >> 4) * 131072 + 1024 + vvo;
      const u32x4 k0 = pk8(acc[0], 0), k1 = pk8(acc[0], 8), v0 = pk8(acc[1], 0), v1 = pk8(acc[1], 8);
      st16sc1(k0, rws, ko, kb); st16sc1(k1, rws, ko, kb + 131072);
      st16sc1(v0, rws, vo, kb); st16sc1(v1, rws, vo, kb + 512);
      ST(4);
      VMW(4);
      ST(3);
      gsync(C_Q, 4, lane);
      ST(7);
      const unsigned zk = XS + w * 16384 + 4096 + ko, zv = XS + w * 16384 + 4096 + (r >> 4) * 8192 + 1024 + vvo;
      ldsw<u32x4>(zk, k0); ldsw<u32x4>(zk + 8192, k1); ldsw<u32x4>(zv, v0); ldsw<u32x4>(zv + 512, v1);
    }
    unsigned kw = 0;
    {
      unsigned qa = RING + (8 * hh + q4) * 64 + ((lane >> 4) & 1) * 32 + (lane & 3) * 8;
      OPQ(qa);
      f32x16 aq = {0}, aq2 = {0};
      unsigned arr = 0;
      u32x4 fq[QD + 1][4];
#define QRD(i, ks) fq[(i) % (QD + 1)][ks] = cat(ldtr(qa + ((i) >> 1) * 12288 + (((i) & 1) ? 0 : 8192) + (ks) * 1024), ldtr(qa + ((i) >> 1) * 12288 + (((i) & 1) ? 0 : 8192) + (ks) * 1024 + 256))
#pragma unroll
      for (int i = 0; i < QD; ++i) { QRD(i, 0); QRD(i, 1); QRD(i, 2); QRD(i, 3); }
      SB();
#pragma unroll
      for (int i = 0; i < 8; ++i) {
#pragma unroll
        for (int ks = 0; ks < 4; ++ks) {
          if (i + QD < 8) QRD(i + QD, ks);
          SB();
          if (Q2 && (ks & 1)) { aq2 = mfma32(fq[i % (QD + 1)][ks], xf[i][ks], aq2); OPQ(aq2); } else { aq = mfma32(fq[i % (QD + 1)][ks], xf[i][ks], aq); OPQ(aq); }
          SB();
          if (4 * i + ks == QFLAG_AT) {
            ST(5);
            asm volatile("s_waitcnt vmcnt(0)\n\ts_mov_b64 exec, 1\n\tds_add_rtn_u32 %0, %1, %2\n\ts_mov_b64 exec, -1" : "+v"(arr) : "v"(C_KVD), "v"(1u) : "memory");
            if (KWARM) asm volatile("buffer_load_dword %0, %1, %2, %3 offen sc1" : "=v"(kw) : "v"(hh * 131072 + r * 64), "s"(rws), "s"(kb) : "memory");
          }
          if (4 * i + ks == QFLAG_AT + 2) {
            unsigned c;
            const u64 tv = tag;
            asm volatile("s_waitcnt lgkmcnt(%6)\n\tv_readfirstlane_b32 %0, %1\n\ts_nop 0\n\ts_cmp_eq_u32 %0, 3\n\ts_cbranch_scc0 1f\n\t"
                         "s_mov_b64 exec, 1\n\tglobal_store_dwordx2 %2, %3, %4 sc1\n\ts_mov_b64 exec, -1\n1:"
                         : "=&s"(c) : "v"(arr), "v"(0u), "v"(tv), "s"(ws + WS_KVFLAG + (hg * 32 + rt) * FS), "n"(0), "n"(QFLAG_AT + 2 < 32 - 4 * QD ? 4 : QFLAG_AT + 1 < 32 - 4 * QD ? 2 : 0) : "memory", "scc");
            ST(6);
          }
        }
      }
      if (Q2) aq += aq2;
      aq *= 0.25f * 1.44269504088896341f;
      ldsw<u32x4>(rg + 4096 + kvo, pk8(aq, 0)); ldsw<u32x4>(rg + 4096 + 1024 + kvo, pk8(aq, 8));
    }
    arrive(C_Q, lane);
    KVINIT();
    ST(19);
    KVFIRST(FCM);
    ATT_BEGIN();
    asm volatile("s_setprio %0" ::"n"(PM_0));
    STIT();
    HALF0(FCM, asm volatile("" ::"v"(kw)), 0);
    HALF(1, NOCV, WOLD(), 1);
    it = 1;
    waitge(C_RDY, 1);
    STFL();
    {
      const unsigned pfo = ((lane & 7) * 32 + (ort & ~1u) + ((lane >> 3) & 1)) * 512 + (lane >> 4) * 128;
      u64 pfl = 0;
      unsigned lvl = PM_LVL;
#pragma unroll 1
      do {
        if ((lvl & 3) == 3) asm volatile("s_setprio 3"); else if ((lvl & 3) == 2) asm volatile("s_setprio 2"); else if ((lvl & 3) == 1) asm volatile("s_setprio 1"); else asm volatile("s_setprio 0");
        lvl >>= 4;
        if (it == 7 && w == 0) asm volatile("global_load_dwordx2 %0, %1, %2 sc1" : "+v"(pfl) : "v"(pfo), "s"(ws + WS_YFLAG) : "memory");
#pragma unroll 1
        do {
          ITER(NOCV, , 0, , 0);
        } while (!((PM_SEG >> it) & 1));
      } while (it < 8);
      if (w == 0) {
        asm volatile("" : "+v"(pfl));
        if (__builtin_amdgcn_ballot_w64(pfl != tag) != 0) pollflags(ws + WS_YFLAG, pfo, tag);
      }
    }
    o[3] = smf(vb[1], ph, o[3]);
  } else {
    const unsigned v = w & 3, cc = v >> 1, a = v & 1;
    const unsigned d = 64 * xc + 2 * xi + cc;
    const unsigned cb0 = SS + cc * CH, ua = cb0 + O_UA, P = ua, cl = C_L + cc * 4;
    asm volatile("s_setprio %0" ::"n"(LPRIO0));
    unsigned pa[6], pl, pd;
    {
      const unsigned po = (d * 128 + 64 * a + lane) * 2, pdo = d * 2;
      asm volatile("global_load_ushort %0, %1, %2" : "=v"(pl) : "v"(pdo), "s"(pLdt) : "memory");
      asm volatile("global_load_ushort %0, %1, %2" : "=v"(pa[0]) : "v"(po), "s"(pAre) : "memory");
      asm volatile("global_load_ushort %0, %1, %2" : "=v"(pa[1]) : "v"(po), "s"(pAim) : "memory");
      asm volatile("global_load_ushort %0, %1, %2" : "=v"(pa[2]) : "v"(po), "s"(pBre) : "memory");
      asm volatile("global_load_ushort %0, %1, %2" : "=v"(pa[3]) : "v"(po), "s"(pBim) : "memory");
      asm volatile("global_load_ushort %0, %1, %2" : "=v"(pa[4]) : "v"(po), "s"(pCre) : "memory");
      asm volatile("global_load_ushort %0, %1, %2" : "=v"(pa[5]) : "v"(po), "s"(pCim) : "memory");
      asm volatile("global_load_ushort %0, %1, %2" : "=v"(pd) : "v"(pdo), "s"(pDsk) : "memory");
    }
    unsigned pf0;
    {
      u64 pc;
      unsigned len;
      asm volatile("s_getpc_b64 %0\n.Lpf_here:\n\ts_mov_b32 %1, .Lpf_end-.Lpf_here" : "=s"(pc), "=s"(len));
      const char* q = (const char*)pc + min(((xi & 3) * 80 + v * 20 + min(lane, 19)) * 128, len);
      asm volatile("global_load_dword %0, %1, off" : "=v"(pf0) : "v"(q) : "memory");
      TAGLOAD();
      spull<4, 3072>((const char*)pW + (xi * 16 + v * 4) * 3072 + hg * 128);
      if (UPF) spull<8, 2048>((const char*)pu + (64 * xi + 16 * v) * 1024 + xc * 128);
    }
    const unsigned ul0 = cb0 + 8704 + a * 2304;
#define ULOAD()                                                                                       \
  {                                                                                                   \
    const rsrc_t ru = mkrs(pu, 2048 * 512 * 2);                                                       \
    const unsigned uo = (64 * xi + 8 * v + (lane >> 3)) * 1024 + xc * 128 + (lane & 7) * 16;          \
    asm volatile("s_setprio 3");                                                                      \
    GLDS(ul0, uo, ru, 0u); GLDS(ul0 + 1024, uo, ru, 32768u);                                          \
    asm volatile("s_setprio %0" ::"n"(KPRIO));                                        \
  }
#define WLOAD() { asm volatile("s_setprio %0" ::"n"(WPRIO)); LDW(3); LDW(4); LDW(5); LDW(6); LDW(7); asm volatile("s_setprio %0" ::"n"(KPRIO)); }
#define WFLAG(n) { ST(24); VMW(n); TAGWAIT(); arrive_nw(C_W); ST(25); }
#define POS(p)                                                                                        \
  if (WFP == (p)) WFLAG((UPOS >= WPOS && UPOS < WFP) ? 2 : 0);                                        \
  if (WPOS == (p)) WLOAD();                                                                           \
  if (UPOS == (p)) ULOAD();
    POS(0);
    ST(1);
    asm volatile("s_waitcnt vmcnt(%8)" : "+v"(pa[0]), "+v"(pa[1]), "+v"(pa[2]), "+v"(pa[3]), "+v"(pa[4]), "+v"(pa[5]), "+v"(pl), "+v"(pd) : "n"(1 + (UPOS == 0 ? 2 : 0) + (WPOS == 0 ? 10 : 0)) : "memory");
    if (UPF) spull<8, 2048>((const char*)pu + (64 * xi + 16 * v + 1) * 1024 + xc * 128);
    ST(2);
    POS(1);
    ST(23);
    asm volatile("s_setprio %0" ::"n"(KPRIO));
    const float dsk = lo16f(pd);
    {
      const float dt = rbf(expf(lo16f(pl)));
      float a2 = dt * lo16f(pa[0]) * 1.44269504088896341f, br = dt * lo16f(pa[1]) * 0.15915494309189535f;
      cf c = cmul(cf{lo16f(pa[4]), -lo16f(pa[5])}, cf{dt * lo16f(pa[2]), -dt * lo16f(pa[3])});
      cf e128 = cexpl(a2, br, 128.f), e1k = cexpl(a2, -br, 1024.f);
      const cf g2k = cmul(c, cmul(e1k, e1k));
      unsigned p2k = pk2(g2k[0], g2k[1]);
      const unsigned row = lane & 15;
      const float ll = (float)(8 * row), lh = (float)(((0x4040642076543210ull >> (4 * row)) & 7) + 256 * ((0xFA550000u >> (2 * row)) & 3));
      u32x4 h0[4], h1[4], lf[2][4];
#define BC(j) " row_newbcast:" j " row_mask:0xf bank_mask:0xf\n\t"
#define BARG(j)                                                                                       \
  asm volatile("v_mul_f32_dpp %0, %4, %6" BC("%8") "v_mul_f32_dpp %1, %5, %6" BC("%8") "v_mul_f32_dpp %2, %4, %7" BC("%8") "v_mul_f32_dpp %3, -%5, %7" BC("%8") \
               : "=&v"(ea), "=&v"(pha), "=&v"(eb), "=&v"(phb) : "v"(a2), "v"(br), "v"(ll), "v"(lh), "n"(j))
      float ea, pha, eb, phb, gr = 0.f, gi = 0.f;
      asm volatile("s_nop 1" : "+v"(a2), "+v"(br), "+v"(c), "+v"(e128), "+v"(e1k), "+v"(p2k), "+v"(gr), "+v"(gi) : "v"(ll), "v"(lh));
      BARG(0);
      POS(2);
#pragma unroll
      for (int j = 0; j < 17; ++j) {
        ea = __builtin_amdgcn_exp2f(ea); pha = __builtin_amdgcn_fractf(pha); eb = __builtin_amdgcn_exp2f(eb); phb = __builtin_amdgcn_fractf(phb);
        const float xr = j < 16 ? ea * __builtin_amdgcn_cosf(pha) : gr, xi_ = j < 16 ? ea * __builtin_amdgcn_sinf(pha) : gr;
        const float yr = j < 16 ? eb * __builtin_amdgcn_cosf(phb) : gr, yi = j < 16 ? eb * __builtin_amdgcn_sinf(phb) : gr;
        float x1r, x1i, nr, ni, g1r, g1i;
        SB();
        if (j < 15) BARG(j + 1); else asm volatile("s_nop 1");
        asm volatile("v_mul_f32_dpp %0, %6, %12" BC("%18") "v_mul_f32_dpp %1, %6, %13" BC("%18") "v_mul_f32_dpp %2, %8, %14" BC("%18")
                     "v_mul_f32_dpp %3, %8, %15" BC("%18") "v_mul_f32_dpp %4, %10, %16" BC("%19") "v_mul_f32_dpp %5, %10, %17" BC("%19")
                     "v_fmac_f32_dpp %0, -%7, %13" BC("%18") "v_fmac_f32_dpp %1, %7, %12" BC("%18") "v_fmac_f32_dpp %2, -%9, %15" BC("%18")
                     "v_fmac_f32_dpp %3, %9, %14" BC("%18") "v_fmac_f32_dpp %4, -%11, %17" BC("%19") "v_fmac_f32_dpp %5, %11, %16" BC("%19")
                     : "=&v"(x1r), "=&v"(x1i), "=&v"(nr), "=&v"(ni), "=&v"(g1r), "=&v"(g1i)
                     : "v"(e128[0]), "v"(e128[1]), "v"(c[0]), "v"(c[1]), "v"(e1k[0]), "v"(e1k[1]), "v"(xr), "v"(xi_), "v"(yr), "v"(yi),
                       "v"(gr), "v"(gi), "n"(j & 15), "n"((j - 1) & 15));
        SB();
        if (j < 16) { lf[0][j >> 2][j & 3] = pk2(xr, xi_); lf[1][j >> 2][j & 3] = pk2(x1r, x1i); h0[j >> 2][j & 3] = pk2(nr, ni); }
        if (j > 0) h1[(j - 1) >> 2][(j - 1) & 3] = pk2(g1r, g1i);
        gr = nr; gi = ni;
        SB(); POS(3 + j); SB();
      }
#pragma unroll
      for (int j = 0; j < 16; ++j) {
        unsigned t1 = h1[j >> 2][j & 3];
        asm volatile("v_mov_b32_dpp %0, %1 row_newbcast:%2 row_mask:0xf bank_mask:0x2" : "+v"(t1) : "v"(p2k), "n"(j));
        h1[j >> 2][j & 3] = t1;
      }
      f32x4 dd[2][2];
#pragma unroll
      for (int lt = 0; lt < 2; ++lt)
        asm volatile(
            "s_nop 1\n\tv_mfma_f32_16x16x32_bf16 %0, %2, %6, 0\n\tv_mfma_f32_16x16x32_bf16 %1, %2, %10, 0\n\t"
            "v_mfma_f32_16x16x32_bf16 %0, %3, %7, %0\n\tv_mfma_f32_16x16x32_bf16 %1, %3, %11, %1\n\t"
            "v_mfma_f32_16x16x32_bf16 %0, %4, %8, %0\n\tv_mfma_f32_16x16x32_bf16 %1, %4, %12, %1\n\t"
            "v_mfma_f32_16x16x32_bf16 %0, %5, %9, %0\n\tv_mfma_f32_16x16x32_bf16 %1, %5, %13, %1\n\ts_nop 7"
            : "=&v"(dd[lt][0]), "=&v"(dd[lt][1])
            : "v"(lf[lt][0]), "v"(lf[lt][1]), "v"(lf[lt][2]), "v"(lf[lt][3]), "v"(h0[0]), "v"(h0[1]), "v"(h0[2]), "v"(h0[3]), "v"(h1[0]),
              "v"(h1[1]), "v"(h1[2]), "v"(h1[3])
            : "memory");
      POS(20);
      ST(26);
      waitge(C_S, 4);
      ST(27);
      POS(21);
      asm volatile("s_setprio %0" ::"n"(EPRIO));
      const unsigned pw = P + a * 3024 + row * 144 + (lane >> 4) * 16;
      const unsigned pw1 = P + a * 3024 + (row == 4 ? 20 : 16 + __builtin_popcount(row)) * 144 + (lane >> 4) * 16;
      ldsw<f32x4>(pw, dd[0][0]); ldsw<f32x4>(pw + 64, dd[1][0]);
      asm volatile("s_mov_b64 exec, %3\n\tds_write_b128 %0, %1\n\tds_write_b128 %0, %2 offset:64\n\ts_mov_b64 exec, -1"
                   ::"v"(pw1), "v"(dd[0][1]), "v"(dd[1][1]), "s"(0x5111511151115111ull) : "memory");
    }
    {
      asm volatile("s_waitcnt vmcnt(0)" : "+v"(pf0)::"memory");
      gsync(C_U, 4, lane);
      const unsigned ta = SS + (lane >> 5) * CH + 8704 + ((lane >> 4) & 1) * 2304 + ((lane & 15) >> 2) * 128 + v * 32 + (lane & 3) * 8;
      u32x4 t0 = cat(ldtr(ta), ldtr(ta + 512)), t1 = cat(ldtr(ta + 1024), ldtr(ta + 1536));
      OPQ(t0); OPQ(t1);
      arrive_nw(C_U);
      const unsigned tw = cb0 + a * 2304 + (lane & 15) * 144 + (lane >> 4) * 16, tr = cb0 + a * 2304 + (lane >> 3) * 144 + (lane & 7) * 16;
      ldsw<u32x4>(tw, t0); ldsw<u32x4>(tw + 64, t1);
      const unsigned go = (lane >> 3) * 4096 + (lane & 7) * 16, gs = WS_UT + (64 * xc + 16 * v) * 4096 + xi * 128;
      if (!XA) { st16sc1(ldsr<u32x4>(tr), rws, go, gs); st16sc1(ldsr<u32x4>(tr + 8 * 144), rws, go, gs + 8 * 4096); }
    }
    ST(3);
    gsync(cl, 2, lane);
    waitge(C_U, 8);
    {
      u32x4 dk;
      unsigned f0 = 0, f1 = 0;
      const unsigned lo = lane & 31;
#define RDP(ad) (ldsr<float>(ad) + ldsr<float>((ad) + 3024))
      const unsigned n = 4 * lo + 1 + a + 2 * hh;
      const float tn = RDP(P + (16 + (n >> 5)) * 144 + (n & 31) * 4);
      if (a == 0) {
        const unsigned b0 = P + lo * 4 + hh * 8 * 144, h1 = hh * 144;
        float t[8];
        t[0] = RDP(b0);
        t[1] = RDP(b0 + 144 - h1);
        t[2] = RDP(b0 + 2 * 144 - h1);
        t[3] = RDP(b0 + 3 * 144 - h1);
        t[4] = RDP(b0 + 4 * 144 - 2 * h1);
        t[5] = RDP(b0 + 5 * 144 - 3 * h1);
        t[6] = RDP(b0 + 6 * 144 - 3 * h1);
        t[7] = RDP(b0 + (hh ? (lo < 31 ? 4 : 20 + 3 * 144) : 7 * 144));
        dk = u32x4{pk2(t[7], t[6]), pk2(t[5], t[4]), pk2(t[3], t[2]), pk2(t[1], t[0])};
      } else {
        const unsigned b1 = P + (12 + 2 * hh) * 144 + lo * 4;
        float v0 = RDP(b1), v4 = RDP(b1 + 144), v8 = RDP(b1 + (lo < 31 ? 4 : 20 + 144)), k510 = RDP(P + 11 * 144 + 124), k508 = RDP(P + 10 * 144 + 124);
        dk = u32x4{pk2(v8, v8), pk2(v4, v4), pk2(v4, v0), pk2(v0, v0)};
        f0 = pk2(v0, k510); f1 = pk2(k508, k508);
      }
      arrive(cl, lane);
      ldsw<u32x4>(cb0 + (lane >> 4) * 4160 + a * 512 + (lane & 8) * 32 + 64 + (lane & 7) * 16, u32x4{0, 0, 0, 0});
      ldsw<u32x4>(cb0 + ((lane >> 2) & 3) * 4160 + 4096 + (lane & 3) * 16, u32x4{0, 0, 0, 0});
      const u16 tb = (u16)pk2(tn, tn);
#pragma unroll
      for (int q = 0; q < 4; ++q) ldsw<u16>(cb0 + a * 512 + hh * 256 + 70 - 2 * lo + q * 4158, tb);
      unsigned o = cb0 + (255 - 64 * a - lane) * 16;
#pragma unroll 1
      for (unsigned k = 0; k <= a; ++k) {
        const unsigned n0 = __builtin_amdgcn_update_dpp(f0, dk[0], 0x138, 0xf, 0xf, false), n1 = __builtin_amdgcn_update_dpp(f1, dk[1], 0x138, 0xf, 0xf, false);
        unsigned e0 = __builtin_amdgcn_alignbit(dk[1], dk[0], 16), e1 = __builtin_amdgcn_alignbit(dk[2], dk[1], 16),
                 e2 = __builtin_amdgcn_alignbit(dk[3], dk[2], 16), e3 = __builtin_amdgcn_alignbit(n0, dk[3], 16),
                 e4 = __builtin_amdgcn_alignbit(n1, n0, 16);
        ldsw<u32x4>(o, dk);
        ldsw<u32x4>(o + 4160, u32x4{e0, e1, e2, e3});
        ldsw<u32x4>(o + 8320, u32x4{dk[1], dk[2], dk[3], n0});
        ldsw<u32x4>(o + 12480, u32x4{e1, e2, e3, e4});
        f0 = __builtin_amdgcn_readlane(dk[0], 63); f1 = __builtin_amdgcn_readlane(dk[1], 63);
        const unsigned z = lane == 0 ? f0 : 0u;
        dk = u32x4{0, z & 0xffff0000u, z, z};
        o -= 1024;
      }
    }
    ST(4);
    KVINIT();
    KVFIRST(FCL);
    ST(6);
    ATT_BEGIN();
    {
      asm volatile("s_setprio %0" ::"n"(PL_0));
      u64 f0;
      const unsigned ufo = (xc * 32 + r) * 512 + (xi >> 3) * 128, kfo = (hg * 32 + (r & 15) * 2 + sv) * FS;
      STIT();
      HALF0(FCL, putflag(ws + WS_UFLAG + (xc * 32 + xi) * 512 + v * 128, tag), 1);
      HALF(1, NOCV, FLAGLD(f0, kfo, ws + WS_KVFLAG); WOLD(), 1);
      FLAGCK(f0, kfo, ws + WS_KVFLAG);
      arrive_nw(C_RDY);
      it = 1;
      asm volatile("s_setprio %0" ::"n"(PL_1));
      {
        u32x4 u0, u1, bx[3];
#define UCV(n, g)                                                                                     \
  if ((n) == 1 && (g) == 1) { SB(); FLAGLD(f0, ufo, ws + WS_UFLAG); SB(); }                           \
  if ((n) == 1 && (g) == 5) {                                           \
    SB();                                                                                             \
    const u64 m = (u64)(a ? 0x1ffu : 0x7fu) << (r + 5 - a);                                           \
    const unsigned ml = (unsigned)(m >> (8 * hh)), mh = (unsigned)(m >> 32) >> (8 * hh);              \
    _Pragma("unroll") for (int t = 0; t < 3; ++t) {                                                   \
      const unsigned m8 = (t == 2 ? mh : ml >> (16 * t)) & 0xff, z = m8 | (m8 << 15);                 \
      _Pragma("unroll") for (int j = 0; j < 4; ++j) bx[t][j] = (z & (0x10001u << (2 * j))) * (0x3f80u >> (2 * j)); \
      OPQ(bx[t]);                                                                                     \
    }                                                                                                 \
    waitge(cl, 4);                                                                                    \
    ldsw<u32x4>(ua + (lane & 3) * 16, u32x4{0, 0, 0, 0});                                             \
    SB();                                                                                             \
  }                                                                                                   \
  if ((n) == 3 && (g) == 6) {                                                                         \
    SB();                                                                                             \
    asm volatile("s_waitcnt vmcnt(0)" : "+v"(u0), "+v"(u1)::"memory");                                \
    const unsigned uw = ua + (32 * a + (lane >> 2) + 1) * 80 + (lane & 3) * 16;                       \
    ldsw<u32x4>(uw, u0); ldsw<u32x4>(uw + 16 * 80, u1);                                               \
    arrive_nw(cl);                                                                                    \
    SB();                                                                                             \
  }
        ITER(UCV, , -1, FLAGCK(f0, ufo, ws + WS_UFLAG);
             asm volatile("buffer_load_dwordx4 %0, %2, %3, %4 offen sc1\n\tbuffer_load_dwordx4 %1, %2, %3, %4 offen offset:1024 sc1"
                          : "=&v"(u0), "=&v"(u1) : "v"(kvo), "s"(rws), "s"(WS_UT + d * 4096 + a * 2048) : "memory"), 0);
        ST(7);
        asm volatile("s_setprio %0" ::"n"(PL_C));
        waitge(cl, 6);
        ST(8);
#define LDV2(ad) (*(const volatile AS3 u32x2*)(ad))
        f32x16 d0, d1;
        const unsigned cq = cb0 + (3 - (r & 3)) * 4160 - 8 * (r >> 2);
        {
          { const u32x4 z = ldsr<u32x4>(ua); d0 = mfma32(z, z, zero16); }
          const unsigned ax = cq + 2040 + 16 * hh - 64 * a, ux = ua + 16 * hh + 80 * (r + 1 - a);
          d1 = mfma32(cat(LDV2(ax), LDV2(ax + 8)), ldsr<u32x4>(ux), zero16);
          d1 = mfma32(cat(LDV2(ax + 32), LDV2(ax + 40)), ldsr<u32x4>(ux + 32), d1);
          u32x4 uc[4];
#pragma unroll
          for (int t = 0; t < 4; ++t) { const int e = 2 * t - 4 + (int)hh - (int)a; uc[t] = ldsr<u32x4>(ua + 80 * (r + 1) + 16 * e + 16 * (e >> 2)); }
          f32x16 x0 = mfma32(uc[1], bx[0], zero16), x1 = mfma32(uc[0], bx[0], zero16);
          x0 = mfma32(uc[2], bx[1], x0); x1 = mfma32(uc[1], bx[1], x1);
          x0 = mfma32(uc[3], bx[2], x0); x1 = mfma32(uc[2], bx[2], x1);
          const unsigned ta = cq + a * 512 + 64 + 8 * hh;
          d1 = mfma32(pk8(x0, 0), cat(LDV2(ta), LDV2(ta + 16)), d1);
          d1 = mfma32(pk8(x0, 8), cat(LDV2(ta + 32), LDV2(ta + 48)), d1);
          d1 = mfma32(pk8(x1, 0), cat(LDV2(ta + 256), LDV2(ta + 272)), d1);
          d1 = mfma32(pk8(x1, 8), cat(LDV2(ta + 288), LDV2(ta + 304)), d1);
        }
        unsigned ab = cq + 4088 + 16 * hh - 1024 * a - 512;
        unsigned uh = ua + 16 * hh + 80 * (r + 33 - 16 * a - 8);
        const int ub = (int)(ua + 16 * hh);
        int ut = ub + 80 * (int)(r + 1) - 1280 * (int)a;
        unsigned ul = (unsigned)max(ut, ub);
        OPQ(ab); OPQ(uh); OPQ(ul);
        u32x4 al[2], bh[2], bl[2];
#pragma unroll
        for (int k = 0; k < 2; ++k) { al[k] = cat(LDV2(ab + 512 + 32 * k), LDV2(ab + 520 + 32 * k)); bh[k] = ldsr<u32x4>(uh + 640 + 32 * k); bl[k] = ldsr<u32x4>(ul + 32 * k); }
        ST(22);
#define CONV(n, g)                                                                                    \
  SB();                                                                                               \
  if (!((n) & 1)) { d1 = mfma32(al[(n) >> 1], bh[(n) >> 1], d1); SB(); bh[(n) >> 1] = ldsr<u32x4>(uh + (7 - (g)) * 80 + 16 * (n)); } \
  if ((n) == 0) { ut -= 80; ul = (unsigned)max(ut, ub); OPQ(ul); }                                    \
  if ((n) & 1) {                                                                                      \
    d0 = mfma32(al[(n) >> 1], bl[(n) >> 1], d0); SB();                                                \
    al[(n) >> 1] = cat(LDV2(ab + (7 - (g)) * 64 + 32 * ((n) >> 1)), LDV2(ab + (7 - (g)) * 64 + 32 * ((n) >> 1) + 8)); \
    bl[(n) >> 1] = ldsr<u32x4>(ul + 32 * ((n) >> 1));                                                 \
  }                                                                                                   \
  SB();
#pragma unroll 1
        do {
          ITER(CONV, , 0, , 0);
          ab -= 512; uh -= 640; OPQ(ab); OPQ(uh);
        } while (it < 4);
        ST(12);
#pragma unroll
        for (int j = 0; j < 4; ++j) { ldsw<f32x4>(RING + v * 12288 + j * 1024 + kvo, sub4(d0, 4 * j)); ldsw<f32x4>(RING + v * 12288 + 8192 + j * 1024 + kvo, sub4(d1, 4 * j)); }
        arrive_nw(cl);
        asm volatile("s_setprio %0" ::"n"(PL_4));
        ST(18);
        f32x4 o1[4], o2[4];
        u32x2 uu[4];
#define YCV(n, g)                                                                                     \
  if ((n) == 1 && (g) == 1) {                                                                         \
    SB();                                                                                             \
    waitge(cl, 8);                                                                                    \
    _Pragma("unroll") for (int j = 0; j < 4; ++j) {                                                   \
      o1[j] = ldsr<f32x4>(RING + cc * 24576 + a * 8192 + j * 1024 + kvo);                             \
      o2[j] = ldsr<f32x4>(RING + cc * 24576 + a * 8192 + 12288 + j * 1024 + kvo);                     \
      uu[j] = ldsr<u32x2>(ua + (32 * a + r + 1) * 80 + j * 16 + hh * 8);                              \
    }                                                                                                 \
    arrive_nw(C_Y);                                                                                   \
    SB();                                                                                             \
  }                                                                                                   \
  if (((n) & 1) && (g) == 2) {                                                                        \
    SB();                                                                                             \
    u32x4 yt;                                                                                         \
    _Pragma("unroll") for (int j = (n) - 1; j <= (n); ++j) {                                          \
      float uf[4] = {lo16f(uu[j][0]), hi16f(uu[j][0]), lo16f(uu[j][1]), hi16f(uu[j][1])}, y[4];       \
      _Pragma("unroll") for (int e = 0; e < 4; ++e) {                                                 \
        unsigned ys = pk2(o1[j][e] + o2[j][e], uf[e] * dsk);                                          \
        y[e] = lo16f(ys) + hi16f(ys);                                                                 \
      }                                                                                               \
      yt[2 * (j & 1)] = pk2(y[0], y[1]); yt[2 * (j & 1) + 1] = pk2(y[2], y[3]);                       \
    }                                                                                                 \
    st16sc1(yt, rws, kvo, WS_UT + d * 4096 + a * 2048 + ((n) >> 1) * 1024);                           \
    SB();                                                                                             \
  }
        ITER(YCV, POISON(0); POISON(-2), 4, , 0);
        asm volatile("s_setprio %0" ::"n"(PL_5));
      }
      const unsigned yfo = (xc * 32 + r) * 512 + hh * 256 + (xi >> 4) * 128;
      f0 = 0;
#pragma unroll 1
      do {
        PRIO_AT(6, PL_6);
        if (it == 7) asm volatile("global_load_dwordx2 %0, %1, %2 sc1\n\ts_setprio %3" : "+v"(f0) : "v"(yfo), "s"(ws + WS_YFLAG), "n"(PL_LAST) : "memory");
        ITER(NOCV, putflag(ws + (it == 5 ? WS_YFLAG : WS_UFLAG) + (xc * 32 + xi) * 512 + v * 128, tag), 1, , 0);
      } while (it < 8);
      FLAGCK(f0, yfo, ws + WS_YFLAG);
      waitge(C_Y, 4);
#pragma unroll
      for (int h = 0; h < 2; ++h)
        asm volatile("s_mov_b32 m0, %0\n\ts_nop 0\n\tbuffer_load_dwordx4 %1, %2, %3 offen sc1 lds"
                     ::"s"(YL + (2 * v + h) * 2304), "v"((lane >> 1) * 4096 + (lane & 1) * 16), "s"(rws),
                       "s"(WS_UT + (64 * xc + 32 * h) * 4096 + (xi >> 4) * 2048 + v * 512 + (xi & 15) * 32) : "memory");
      STFL();
    }
    o[3] = smf(vb[1], ph, o[3]);
  }

  ST(10);
  {
    unsigned ow = RING + hs * 36864 + kq * 2304 + kvo, orr = RING + hs * 36864 + kq * 9216 + kvo, lw = ow + 2048 - lane * 12, lr = orr + 2048 - lane * 12;
    OPQ(ow); OPQ(orr); OPQ(lw); OPQ(lr);
    f32x8 ov8;
    float l;
    waitge(C_Y, 4);
#pragma unroll
    for (int t = 0; t < 4; ++t) { ldsw<f32x4>(ow + t * 9216, sub4(o[t], 0)); ldsw<f32x4>(ow + t * 9216 + 1024, sub4(o[t], 4)); ldsw<float>(lw + t * 9216, o[t][8]); }
    BAR();
    {
      f32x4 a0[4], a1[4];
      float ls[4];
#pragma unroll
      for (int s = 0; s < 4; ++s) { ls[s] = ldsr<float>(lr + s * 2304); }
#pragma unroll
      for (int s = 0; s < 4; ++s) { a0[s] = ldsr<f32x4>(orr + s * 2304); a1[s] = ldsr<f32x4>(orr + s * 2304 + 1024); }
      l = (ls[0] + ls[1]) + (ls[2] + ls[3]);
#pragma unroll
      for (int j = 0; j < 4; ++j) { ov8[j] = (a0[0][j] + a0[1][j]) + a0[2][j] + a0[3][j]; ov8[4 + j] = (a1[0][j] + a1[1][j]) + a1[2][j] + a1[3][j]; }
    }
    const float am = fmaxf(fmaxf(fmaxf(fabsf(ov8[0]), fabsf(ov8[1])), fmaxf(fabsf(ov8[2]), fabsf(ov8[3]))),
                           fmaxf(fmaxf(fabsf(ov8[4]), fabsf(ov8[5])), fmaxf(fabsf(ov8[6]), fabsf(ov8[7]))));
    if (__builtin_amdgcn_ballot_w64(!(l >= 0x1p-40f && l <= 0x1p40f && am <= 0x1p120f)) != 0) {
      const unsigned kb = WS_KV + head * 131072;
      const u32x4 q = (kq & 2) ? ((kq & 1) ? qf[3] : qf[2]) : ((kq & 1) ? qf[1] : qf[0]);
      float m = -INFINITY;
#pragma unroll 1
      for (int t = 0; t < 64; ++t) {
        f32x16 s = mfma32(__builtin_amdgcn_raw_buffer_load_b128(rws, kvo, kb + t * 2048, 16), q, zero16);
#pragma unroll
        for (int j = 0; j < 16; ++j) m = fmaxf(m, s[j]);
      }
      m = fmaxf(m, xor32(m));
      const unsigned one = (lane & 27) == 16 ? 0x3f803f80u : 0u;
      const u32x4 on4 = {one, one, one, one};
      f32x16 acc = {0};
#pragma unroll 1
      for (int t = 0; t < 64; ++t) {
        f32x16 s = mfma32(__builtin_amdgcn_raw_buffer_load_b128(rws, kvo, kb + t * 2048, 16), q, zero16);
        u32x4 x0 = __builtin_amdgcn_raw_buffer_load_b128(rws, vvo + 1024, kb + t * 2048, 16), x1 = __builtin_amdgcn_raw_buffer_load_b128(rws, vvo + 1536, kb + t * 2048, 16);
        if (lane & 16) { x0 = on4; x1 = on4; }
        f32x8 p0, p1;
#pragma unroll
        for (int j = 0; j < 8; ++j) { p0[j] = __builtin_amdgcn_exp2f(s[j] - m); p1[j] = __builtin_amdgcn_exp2f(s[8 + j] - m); }
        acc = smf(x0, p0, acc); acc = smf(x1, p1, acc);
      }
#pragma unroll
      for (int j = 0; j < 8; ++j) ov8[j] = acc[j];
      l = acc[8];
    }
    const float inv = __builtin_amdgcn_rcpf(l);
    u32x4 ov = {pk2(ov8[0] * inv, ov8[1] * inv), pk2(ov8[2] * inv, ov8[3] * inv), pk2(ov8[4] * inv, ov8[5] * inv), pk2(ov8[6] * inv, ov8[7] * inv)};
#pragma unroll
    for (int j = 0; j < 4; ++j) ov[j] = min(ov[j], 0xFFFFFFFEu);
    st16sc1(ov, rws, kvo, WS_O + ((4 * tp + kq) * 32 + head) * 1024);
  }
  {
    const unsigned qt = w & 1;
    const unsigned q4 = (lane & 15) >> 2, pq = lane & 3;
    const unsigned wa = XS + (128 * kq + 4 * hh + q4) * 128 + ((32 * ((lane >> 4) & 1) + 16 * (pq & 1) + 8 * (pq >> 1)) ^ (((q4 >> 1) & 1) << 6));
    ST(11);
    u32x4 af[2][8];
#pragma unroll
    for (int j = 0; j < 8; ++j) {
      af[0][j] = cat(ldtr(wa + j * 2048), ldtr(wa + j * 2048 + 1024));
      af[1][j] = cat(ldtr((wa ^ 64) + j * 2048), ldtr((wa ^ 64) + j * 2048 + 1024));
    }
    SB();
    u32x4 of[8];
    const unsigned ob = WS_O + ((2 * ort + qt) * 32 + 8 * kq) * 1024;
#pragma unroll
    for (int j = 0; j < 8; ++j) of[j] = __builtin_amdgcn_raw_buffer_load_b128(rws, kvo + j * 1024, ob, 16);
    SB();
    const unsigned yl = YL + w * 2304;
    unsigned ta = YL + kq * 4608 + (lane >> 4) * 256 + (lane & 15) * 8, tw = yl + 1024 + (lane & 7) * 144 + (lane >> 4) * 16, tr = yl + 1024 + (lane >> 3) * 144 + (lane & 7) * 16;
    unsigned to = (8 * (lane >> 5) + ((lane >> 3) & 3)) * 2048 + (lane & 7) * 16;
    unsigned rw_ = XS + qt * 35072 + kq * 2176 + hh * 544 + r * 16;
    unsigned rr_ = XS + qt * 35072 + ((lane & 7) >> 1) * 8768 + (lane & 1) * 544 + (8 * kq + (lane >> 3)) * 16, fo = (lane >> 3) * 2048 + (lane & 7) * 16;
    OPQ(ta); OPQ(tw); OPQ(tr); OPQ(to); OPQ(rw_); OPQ(rr_); OPQ(fo);
    VMW(9);
    BAR();
    {
      const u32x4 y0 = cat(ldtr(ta), ldtr(ta + 128)), y1 = cat(ldtr(ta + 2304), ldtr(ta + 2432));
      if (((lane >> 3) & 1) == qt) { ldsw<u32x4>(tw, y0); ldsw<u32x4>(tw + 64, y1); }
    }
    u32x4 yr = ldsr<u32x4>(tr);
    OPQ(yr);
    const unsigned yo = (64 * xi + 32 * qt + 16 * (kq >> 1) + 4 * (kq & 1)) * 2048 + xc * 128;
    st16(yr, rout, to, yo);
    f32x16 acc0 = {0}, acc1 = {0};
#pragma unroll
    for (int g = 0; g < 2; ++g) {
      for (unsigned it = 0; it < (1u << 20); ++it) {
        unsigned m = 0;
#pragma unroll
        for (int j = 4 * g; j < 4 * g + 4; ++j) m = max(m, max(max(of[j][0], of[j][1]), max(of[j][2], of[j][3])));
        if (__builtin_amdgcn_ballot_w64(m == ~0u) == 0) break;
        if (OSL) asm volatile("s_sleep %0" ::"n"(OSL) : "memory");
#pragma unroll
        for (int j = 4 * g; j < 8; ++j) of[j] = __builtin_amdgcn_raw_buffer_load_b128(rws, kvo + j * 1024, ob, 16);
        asm volatile("" ::: "memory");
      }
#pragma unroll
      for (int j = 4 * g; j < 4 * g + 4; ++j) { acc0 = mfma32(af[0][j], of[j], acc0); acc1 = mfma32(af[1][j], of[j], acc1); }
    }
    ST(13);
#pragma unroll
    for (int c = 0; c < 4; ++c) { ldsw<f32x4>(rw_ + c * 8768, sub4(c < 2 ? acc0 : acc1, 8 * (c & 1))); ldsw<f32x4>(rw_ + c * 8768 + 1088, sub4(c < 2 ? acc0 : acc1, 8 * (c & 1) + 4)); }
    BAR();
    f32x8 kp;
    {
      f32x4 a0[4], a1[4];
#pragma unroll
      for (int s = 0; s < 4; ++s) { a0[s] = ldsr<f32x4>(rr_ + s * 2176); a1[s] = ldsr<f32x4>(rr_ + s * 2176 + 1088); }
#pragma unroll
      for (int j = 0; j < 4; ++j) { kp[j] = (a0[0][j] + a0[1][j]) + a0[2][j] + a0[3][j]; kp[4 + j] = (a1[0][j] + a1[1][j]) + a1[2][j] + a1[3][j]; }
    }
    u32x4 ov = {pk2(kp[0], kp[1]), pk2(kp[2], kp[3]), pk2(kp[4], kp[5]), pk2(kp[6], kp[7])};
    st16(ov, rout, fo, (64 * ort + 32 * qt + 8 * kq) * 2048 + 1024 + oct * 128);
    asm volatile(".Lpf_end:");
    if (blockIdx.x == 0 && tid == 0) setflag(ws + WS_EPOCH, tag);
  }
}

extern "C" void kernel_launch(void* const* d_in, const int* in_sizes, int n_in, void* d_out, int out_size, void* d_ws, size_t ws_size,
                              hipStream_t stream) {
  fused_kernel<<<256, 512, 0, stream>>>((const u16*)d_in[0], (const u16*)d_in[1], (const u16*)d_in[2], (const u16*)d_in[3],
                                        (const u16*)d_in[4], (const u16*)d_in[5], (const u16*)d_in[6], (const u16*)d_in[7],
                                        (const u16*)d_in[8], (const u16*)d_in[9], (const u16*)d_in[10], (const u16*)d_in[11],
                                        (u16*)d_out, (char*)d_ws);
}
```
